```python
import torch

def _softmax(x, axis=-1):
    return torch.softmax(x, dim=axis)
def _gelu(x):
    return torch.nn.functional.gelu(x, approximate="tanh")
def _silu(x):
    return torch.nn.functional.silu(x)
def _sigmoid(x):
    return torch.sigmoid(x)
def _layernorm(x, axis=-1, eps=1e-5):
    m = x.mean(dim=axis, keepdim=True)
    v = x.var(dim=axis, keepdim=True, unbiased=False)
    return (x - m) / torch.sqrt(v + eps)
def _scatter_add(tgt, idx, src, axis=0):
    return tgt.clone().index_add_(axis, idx, src)
def _segment_sum(src, seg, n):
    out = torch.zeros((n,) + src.shape[1:], dtype=src.dtype)
    return out.index_add_(0, seg, src)
def _pad(x, pw):
    flat = tuple(v for pair in reversed(pw) for v in pair)
    return torch.nn.functional.pad(x, flat)

kernel_name = 'concat_heads__sliding_block_cached__gqa_block_cached__decode'

def setup_inputs():

    _w = (lambda a: (lambda t: t.to(torch.bfloat16) if t.is_floating_point() else t)(torch.as_tensor(a)))
    def _A_setup():
        B, Sq, D = 1, 1, 640
        Hq, Hkv, Sk, Dh = 32, 32, 4096, 128
        assert Hq % Hkv == 0, (Hq, Hkv)
        return {
            "x": _w((lambda *s: torch.randn(*s, dtype=torch.bfloat16) * 0.2)(B, Sq, D)),
            "k_cache": _w((lambda *s: torch.randn(*s, dtype=torch.bfloat16) * 3.2)(B, Hkv, Sk, Dh)),
            "v_cache": _w((lambda *s: torch.randn(*s, dtype=torch.bfloat16) * 1.6)(B, Hkv, Sk, Dh)),
            "wq": _w((lambda *s: torch.randn(*s, dtype=torch.bfloat16) * 3.2)(D, Hq * Dh) * (D ** -0.5)),
            "wo": _w((lambda *s: torch.randn(*s, dtype=torch.bfloat16) * 1.6)(Hq * Dh, D) * ((Hq * Dh) ** -0.5)),
        }
    _A_in = _A_setup()

    def _B_setup():
        B, Sq, D = 1, 1, 640
        Hq, Hkv, Sk, Dh = 32, 32, 4096, 128
        assert Hq % Hkv == 0, (Hq, Hkv)
        return {
            "x": _w((lambda *s: torch.randn(*s, dtype=torch.bfloat16) * 0.4)(B, Sq, D)),
            "k_cache": _w((lambda *s: torch.randn(*s, dtype=torch.bfloat16) * 3.2)(B, Hkv, Sk, Dh)),
            "v_cache": _w((lambda *s: torch.randn(*s, dtype=torch.bfloat16) * 0.8)(B, Hkv, Sk, Dh)),
            "wq": _w((lambda *s: torch.randn(*s, dtype=torch.bfloat16) * 3.2)(D, Hq * Dh) * (D ** -0.5)),
            "wo": _w((lambda *s: torch.randn(*s, dtype=torch.bfloat16) * 0.8)(Hq * Dh, D) * ((Hq * Dh) ** -0.5)),
        }
    _B_in = _B_setup()

    return {
        "A_x": _A_in["x"],
        "A_k_cache": _A_in["k_cache"],
        "A_v_cache": _A_in["v_cache"],
        "A_wq": _A_in["wq"],
        "A_wo": _A_in["wo"],
        "B_x": _B_in["x"],
        "B_k_cache": _B_in["k_cache"],
        "B_v_cache": _B_in["v_cache"],
        "B_wq": _B_in["wq"],
        "B_wo": _B_in["wo"],
    }

def reference(A_x, A_k_cache, A_v_cache, A_wq, A_wo, B_x, B_k_cache, B_v_cache, B_wq, B_wo):
    def _A_ref(x, k_cache, v_cache, wq, wo):
        B, Sq, D = x.shape
        Hq, Dh = 32, 128
        Hkv, Sk = k_cache.shape[1], k_cache.shape[2]
        W = min(4096, max(256, Sk // 4))
        rep = Hq // Hkv
        q = (lambda x, *d: x.permute(*d))((x @ wq).reshape(B, Sq, Hq, Dh), 0, 2, 1, 3)
        k = (lambda x, n, axis: torch.repeat_interleave(x, n, dim=axis))(k_cache[:, :, Sk - W:, :], rep, axis=1)
        v = (lambda x, n, axis: torch.repeat_interleave(x, n, dim=axis))(v_cache[:, :, Sk - W:, :], rep, axis=1)
        scores = torch.einsum("bhqd,bhkd->bhqk", q, k) * (Dh ** -0.5)
        p = _softmax(scores, axis=-1)
        o = torch.einsum("bhqk,bhkd->bhqd", p, v)
        return (lambda x, *d: x.permute(*d))(o, 0, 2, 1, 3).reshape(B, Sq, Hq * Dh) @ wo

    def _B_ref(x, k_cache, v_cache, wq, wo):
        B, Sq, D = x.shape
        Hq, Dh = 32, 128
        Hkv = k_cache.shape[1]
        rep = Hq // Hkv
        q = (lambda x, *d: x.permute(*d))((x @ wq).reshape(B, Sq, Hq, Dh), 0, 2, 1, 3)
        k = (lambda x, n, axis: torch.repeat_interleave(x, n, dim=axis))(k_cache, rep, axis=1)
        v = (lambda x, n, axis: torch.repeat_interleave(x, n, dim=axis))(v_cache, rep, axis=1)
        scores = torch.einsum("bhqd,bhkd->bhqk", q, k) * (Dh ** -0.5)
        p = _softmax(scores, axis=-1)
        o = torch.einsum("bhqk,bhkd->bhqd", p, v)
        return (lambda x, *d: x.permute(*d))(o, 0, 2, 1, 3).reshape(B, Sq, Hq * Dh) @ wo

    a = _A_ref(A_x, A_k_cache, A_v_cache, A_wq, A_wo)
    b = _B_ref(B_x, B_k_cache, B_v_cache, B_wq, B_wo)
    return torch.concatenate([a, b], axis=-1)
```

```cpp
#include <hip/hip_runtime.h>
#include <hip/hip_bf16.h>
#include <stdint.h>

typedef unsigned short u16;
typedef unsigned int u32;
typedef u32 u32x4 __attribute__((ext_vector_type(4)));

__device__ __forceinline__ float bf2f(u16 b) { return __builtin_bit_cast(float, (u32)b << 16); }
__device__ __forceinline__ float bfr(float f) {
  __bf16 h = (__bf16)f; return (float)h;
}
#define MIMIC 1

__global__ void __launch_bounds__(256) k_q(const u16* xA, const u16* wqA, const u16* xB, const u16* wqB, float* q) {
  int br = blockIdx.y;
  const u16* x = br ? xB : xA; const u16* w = br ? wqB : wqA;
  int n = blockIdx.x * 64 + (threadIdx.x & 63);
  int part = threadIdx.x >> 6;
  float acc = 0.f;
  for (int d = part * 160; d < part * 160 + 160; ++d) acc += bf2f(x[d]) * bf2f(w[(size_t)d * 4096 + n]);
  __shared__ float red[256];
  red[threadIdx.x] = acc;
  __syncthreads();
  if (threadIdx.x < 64) q[br * 4096 + n] = bfr(red[threadIdx.x] + red[threadIdx.x + 64] + red[threadIdx.x + 128] + red[threadIdx.x + 192]);
}

__global__ void __launch_bounds__(256) k_attn(const u16* kA, const u16* vA, const u16* kB, const u16* vB, const float* q, float* part) {
  int b = blockIdx.x;
  int br, h, key0;
  if (b < 256) { br = 0; h = b >> 3; key0 = 3072 + (b & 7) * 128; }
  else { int bb = b - 256; br = 1; h = bb >> 5; key0 = (bb & 31) * 128; }
  const u16* K = (br ? kB : kA) + ((size_t)h * 4096 + key0) * 128;
  const u16* V = (br ? vB : vA) + ((size_t)h * 4096 + key0) * 128;
  __shared__ float sq[128], sp[128], red[256];
  int t = threadIdx.x;
  if (t < 128) sq[t] = q[br * 4096 + h * 128 + t];
  __syncthreads();
  if (t < 128) {
    float s = 0.f;
    for (int d = 0; d < 128; ++d) s += sq[d] * bf2f(K[(size_t)t * 128 + d]);
    s = bfr(bfr(s) * 0.08838834764831845f);
    sp[t] = s;
  }
  __syncthreads();
  float m = -INFINITY;
  for (int i = 0; i < 128; ++i) m = fmaxf(m, sp[i]);
  __syncthreads();
  if (t < 128) sp[t] = __expf(sp[t] - m);
  __syncthreads();
  float l = 0.f;
  for (int i = 0; i < 128; ++i) l += sp[i];
  int d = t & 127, half = t >> 7;
  float acc = 0.f;
  for (int k = half * 64; k < half * 64 + 64; ++k) acc += sp[k] * bf2f(V[(size_t)k * 128 + d]);
  red[t] = acc;
  __syncthreads();
  float* o = part + (size_t)b * 136;
  if (t < 128) o[t] = red[t] + red[t + 128];
  if (t == 0) { o[128] = m; o[129] = l; }
}

__global__ void __launch_bounds__(128) k_comb(const float* part, float* o) {
  int bh = blockIdx.x;
  int br = bh >> 5, h = bh & 31;
  int ns = br ? 32 : 8;
  int base = br ? 256 + h * 32 : h * 8;
  int t = threadIdx.x;
  float M = -INFINITY;
  for (int s = 0; s < ns; ++s) M = fmaxf(M, part[(size_t)(base + s) * 136 + 128]);
  float L = 0.f, a = 0.f;
  for (int s = 0; s < ns; ++s) {
    const float* p = part + (size_t)(base + s) * 136;
    float w = __expf(p[128] - M);
    L += w * p[129]; a += w * p[t];
  }
  o[br * 4096 + h * 128 + t] = bfr(a / L);
}

__global__ void k_out(const float* o, const u16* woA, const u16* woB, __hip_bfloat16* out) {
  int br = blockIdx.y;
  const u16* w = br ? woB : woA;
  int j = blockIdx.x * 64 + (threadIdx.x & 63);
  int part = threadIdx.x >> 6;
  float acc = 0.f;
  for (int i = part * 256; i < part * 256 + 256; ++i) acc += o[br * 4096 + i] * bf2f(w[(size_t)i * 640 + j]);
  __shared__ float red[1024];
  red[threadIdx.x] = acc;
  __syncthreads();
  if (threadIdx.x < 64) {
    float s = 0.f;
    for (int p = 0; p < 16; ++p) s += red[p * 64 + threadIdx.x];
    out[br * 640 + j] = __float2bfloat16(s);
  }
}

extern "C" void kernel_launch(void* const* d_in, const int* in_sizes, int n_in,
                              void* d_out, int out_size, void* d_ws, size_t ws_size,
                              hipStream_t stream) {
  const u16* xA = (const u16*)d_in[0]; const u16* kA = (const u16*)d_in[1]; const u16* vA = (const u16*)d_in[2];
  const u16* wqA = (const u16*)d_in[3]; const u16* woA = (const u16*)d_in[4];
  const u16* xB = (const u16*)d_in[5]; const u16* kB = (const u16*)d_in[6]; const u16* vB = (const u16*)d_in[7];
  const u16* wqB = (const u16*)d_in[8]; const u16* woB = (const u16*)d_in[9];
  float* q = (float*)d_ws; float* o = q + 8192; float* part = o + 8192;
  k_q<<<dim3(64, 2), 256, 0, stream>>>(xA, wqA, xB, wqB, q);
  k_attn<<<1280, 256, 0, stream>>>(kA, vA, kB, vB, q, part);
  k_comb<<<64, 128, 0, stream>>>(part, o);
  k_out<<<dim3(10, 2), 1024, 0, stream>>>(o, woA, woB, (__hip_bfloat16*)d_out);
}
```

```cpp
#include <hip/hip_runtime.h>
#include <hip/hip_bf16.h>
#include <stdint.h>

typedef unsigned int u32;
typedef unsigned short u16;
typedef unsigned long long u64;
typedef u32 u32x2 __attribute__((ext_vector_type(2)));
typedef u32 u32x4 __attribute__((ext_vector_type(4)));
typedef float f32x2 __attribute__((ext_vector_type(2)));
typedef float f32x4 __attribute__((ext_vector_type(4)));
typedef __bf16 bf16x2 __attribute__((ext_vector_type(2)));
typedef __bf16 bf16x8 __attribute__((ext_vector_type(8)));
#define LAS __attribute__((address_space(3)))

#define WIN 6
constexpr int NWAVE = 8, NTHR = NWAVE * 64;
constexpr int OOB = 0x7fffff00;
constexpr int AUX_NT = 2, AUX_SC1 = 16;
constexpr u32 SPIN_CAP = 200000;

constexpr int WS_EPOCH = 0;
constexpr int WS_Q = 4096;
constexpr int WS_P = WS_Q + 64 * 5 * 1024;
constexpr int WS_ML = WS_P + 64 * 8 * 1024;
constexpr int WS_Y = WS_ML + 64 * 8 * 16;
constexpr int WS_END = WS_Y + 640 * 32 * 16;
#define STAMP(i) do { } while (0)

struct Params {
  const u16 *xA, *kA, *vA, *wqA, *woA, *xB, *kB, *vB, *wqB, *woB;
  u32* out;
  void* ws;
};

#define RSRC(ptr, n) __builtin_amdgcn_make_buffer_rsrc((void*)(ptr), 0, (n), 0x00020000)
#define SB() __builtin_amdgcn_sched_barrier(0)
#define WAITVM(N) __builtin_amdgcn_s_waitcnt(((N) & 15) | (7 << 4) | (15 << 8) | (((N) >> 4) << 14))

__device__ __forceinline__ u32x4 ld_nt(__amdgpu_buffer_rsrc_t r, int off) { return __builtin_amdgcn_raw_buffer_load_b128(r, off, 0, AUX_NT); }
__device__ __forceinline__ u32x4 ld_sc1(__amdgpu_buffer_rsrc_t r, int off) {
  asm volatile("" ::: "memory");
  return __builtin_amdgcn_raw_buffer_load_b128(r, off, 0, AUX_SC1);
}
__device__ __forceinline__ void st_sc1(__amdgpu_buffer_rsrc_t r, int off, u32x4 v) { __builtin_amdgcn_raw_buffer_store_b128(v, r, off, 0, AUX_SC1); }
__device__ __forceinline__ float asf(u32 x) { return __builtin_bit_cast(float, x); }
__device__ __forceinline__ u32 asu(float x) { return __builtin_bit_cast(u32, x); }
__device__ __forceinline__ u32 cvtpk(float lo, float hi) {
  f32x2 v = {lo, hi};
  return __builtin_bit_cast(u32, __builtin_convertvector(v, bf16x2));
}
__device__ __forceinline__ float dot2(u32 a, u32 b, float c) {
  return __builtin_amdgcn_fdot2_f32_bf16(__builtin_bit_cast(bf16x2, a), __builtin_bit_cast(bf16x2, b), c, false);
}
template <int CTRL> __device__ __forceinline__ float dpp(float x) {
  return asf((u32)__builtin_amdgcn_mov_dpp((int)asu(x), CTRL, 0xf, 0xf, true));
}
template <int CTRL> __device__ __forceinline__ u32 dppu(u32 x) { return (u32)__builtin_amdgcn_mov_dpp((int)x, CTRL, 0xf, 0xf, true); }
constexpr int XOR1 = 0xB1, XOR2 = 0x4E, XOR7 = 0x141, XOR8 = 0x128, BCAST = 0x150;
__device__ __forceinline__ float x16_sum(float x) {
  auto s = __builtin_amdgcn_permlane16_swap(asu(x), asu(x), false, false);
  return asf(s[0]) + asf(s[1]);
}
__device__ __forceinline__ float x32_sum(float x) {
  auto s = __builtin_amdgcn_permlane32_swap(asu(x), asu(x), false, false);
  return asf(s[0]) + asf(s[1]);
}
__device__ __forceinline__ float x16_max(float x) {
  auto s = __builtin_amdgcn_permlane16_swap(asu(x), asu(x), false, false);
  return fmaxf(asf(s[0]), asf(s[1]));
}
__device__ __forceinline__ float x32_max(float x) {
  auto s = __builtin_amdgcn_permlane32_swap(asu(x), asu(x), false, false);
  return fmaxf(asf(s[0]), asf(s[1]));
}
__device__ __forceinline__ f32x4 mfma(u32x4 a, u32x4 b, f32x4 c) {
  return __builtin_amdgcn_mfma_f32_16x16x32_bf16(__builtin_bit_cast(bf16x8, a), __builtin_bit_cast(bf16x8, b), c, 0, 0, 0);
}
struct Sel { u32 s[4]; };
__device__ __forceinline__ u32x4 afrag(const Sel& sl, u32 X) {
  u32x4 a;
  a.x = __builtin_amdgcn_perm(X, X, sl.s[0]); a.y = __builtin_amdgcn_perm(X, X, sl.s[1]);
  a.z = __builtin_amdgcn_perm(X, X, sl.s[2]); a.w = __builtin_amdgcn_perm(X, X, sl.s[3]);
  return a;
}
struct Tag { u32 lo8; u32 hi; };
__device__ __forceinline__ u32 gpack(float v, const Tag& t) { return __builtin_amdgcn_alignbyte(t.lo8, asu(v) + 0x80u, 1); }
__device__ __forceinline__ u32x4 gpair(float a, float b, const Tag& t) { u32x4 g = {gpack(a, t), t.hi, gpack(b, t), t.hi}; return g; }
__device__ __forceinline__ u32 gbad(u32x4 g, const Tag& t) {
  return (g.y ^ t.hi) | (g.w ^ t.hi) | ((g.x >> 24) ^ t.lo8) | ((g.z >> 24) ^ t.lo8);
}
__device__ __forceinline__ bool none(u32 bad) { return __builtin_amdgcn_ballot_w64(bad != 0u) == 0ull; }
__device__ __forceinline__ float gv(u32 x) { return asf(x << 8); }

constexpr int RSTR = 136;
constexpr int L_RED = 0;
constexpr int L_Q = 3 * NWAVE * RSTR;
constexpr int L_M = L_Q + 64;
constexpr int L_L = L_M + 8;
constexpr int L_FLAG = L_L + 8;
constexpr int L_TOT = L_FLAG + 4;

__device__ __forceinline__ f32x2 wg_reduce(const float* red, int w, int lane) {
  f32x2 v = *(const f32x2*)(red + (lane & 7) * RSTR + 16 * w + 2 * (lane >> 3));
  v.x += dpp<XOR1>(v.x); v.y += dpp<XOR1>(v.y);
  v.x += dpp<XOR2>(v.x); v.y += dpp<XOR2>(v.y);
  v.x += dpp<XOR7>(v.x); v.y += dpp<XOR7>(v.y);
  return v;
}

template <bool ISA>
__device__ __forceinline__ void body(const Params& p, float* lds, const int role, const int h, const int w, const int lane) {
  constexpr int NT = ISA ? 2 : 1;
  constexpr int NQ = 4 * NT;
  constexpr int NU = ISA ? 16 : 22;
  constexpr int br = ISA ? 0 : 1;
  const int g = lane >> 4, c = lane & 15;
  const int tb = ISA ? 0 : (role < 5 ? 1 : 0);
  const int tk = ISA ? (role - 6) * 2 : (role < 5 ? role : 4);
  const int qslot = ISA ? role - 6 : (role < 5 ? role : 2);
  const int split = ISA ? role - 6 : role;
  int u0, nu;
  if (ISA) { u0 = 768 + split * 128; nu = 128; } else { u0 = role * 1024 / 6; nu = (role + 1) * 1024 / 6 - u0; }

  const size_t kvoff = (size_t)h * (4096 * 128) + (size_t)u0 * 512;
  const auto rk = RSRC((ISA ? p.kA : p.kB) + kvoff, nu * 1024);
  const auto rv = RSRC((ISA ? p.vA : p.vB) + kvoff, nu * 1024);
  const auto rq = RSRC((tb ? p.wqB : p.wqA) + (size_t)tk * (128 * 4096) + h * 128, NT * 128 * 8192);
  const auto ro = RSRC((tb ? p.woB : p.woA) + (size_t)h * (128 * 640) + tk * 128, 128 * 1280);
  const auto rx = RSRC((tb ? p.xB : p.xA) + tk * 128, NT * 256);
  const auto rw = RSRC(p.ws, WS_END);

  STAMP(0);
  u32 xd[NQ / 2];
  if constexpr (ISA) { u32x4 t = __builtin_amdgcn_raw_buffer_load_b128(rx, (w * 4 + g) * 16, 0, 0); xd[0] = t.x; xd[1] = t.y; xd[2] = t.z; xd[3] = t.w; }
  else { u32x2 t = __builtin_amdgcn_raw_buffer_load_b64(rx, (w * 4 + g) * 8, 0, 0); xd[0] = t.x; xd[1] = t.y; }
  u32x4 WQ[NQ], WO[NQ], K[NU], V[NU];
  const int qoff = (w * 4 + g) * NQ * 8192 + c * 16;
  const int ooff = (w * 16 + g * 4) * 1280 + c * 16;
  const int koff = w * 1024 + lane * 16;
  auto issue = [&](int i) {
    if (i < NQ) WQ[i] = ld_nt(rq, qoff + i * 8192);
    else if (i < 2 * NQ) { const int t = i - NQ; WO[t] = ld_nt(ro, ooff + (t & 3) * 1280 + (t >> 2) * 256); }
    else if (i < 2 * NQ + NU) K[i - 2 * NQ] = ld_nt(rk, koff + (i - 2 * NQ) * 8192);
    else V[i - 2 * NQ - NU] = ld_nt(rv, koff + (i - 2 * NQ - NU) * 8192);
    SB();
  };
  constexpr int I_K = 2 * NQ, I_V = 2 * NQ + NU, I_END = 2 * NQ + 2 * NU;
#pragma unroll
  for (int i = 0; i < WIN; ++i) issue(i);

  const u64 ep = __builtin_nontemporal_load((const u64*)((const char*)p.ws + WS_EPOCH)) + 1ull;
  Tag tag; tag.lo8 = (u32)ep & 0xffu; tag.hi = (u32)(ep >> 8);
  Sel sel;
  {
    const u32 src = (c & 8) ? 0x0100u : 0x0302u;
    const u32 m = (c & 1) ? ((src << 16) | 0x0c0cu) : (0x0c0c0000u | src);
#pragma unroll
    for (int i = 0; i < 4; ++i) sel.s[i] = (((c & 7) >> 1) == i) ? m : 0x0c0c0c0cu;
  }
  if (threadIdx.x == 0) ((volatile LAS u32*)lds)[L_FLAG] = 0u;
  SB();

  f32x4 acc = {0.f, 0.f, 0.f, 0.f};
#pragma unroll
  for (int t = 0; t < NQ; ++t) {
    const u32 X = (t & 1) ? xd[t >> 1] : (xd[t >> 1] << 16);
    acc = mfma(afrag(sel, X), WQ[t], acc);
    SB();
    issue(WIN + t);
  }
  if (lane < 32) *(f32x4*)(lds + L_RED + w * RSTR + 8 * c + 4 * g) = acc;
  asm volatile("s_waitcnt lgkmcnt(0)\n\ts_barrier" ::: "memory");
  {
    const f32x2 v = wg_reduce(lds + L_RED, w, lane);
    if ((lane & 7) == 0) st_sc1(rw, WS_Q + ((tb * 32 + h) * 5 + qslot) * 1024 + (16 * w + 2 * (lane >> 3)) * 8, gpair(v.x, v.y, tag));
  }
  SB();
  STAMP(1);

  constexpr int NQP = ISA ? 3 : 5;
  constexpr int I_G = I_K + (ISA ? 6 : 10);
  constexpr int I_GC = I_G + WIN;
  constexpr int I_QK = I_V + WIN;
  u32x4 G[NQP];
  const int goff = WS_Q + (br * 32 + h) * 5 * 1024 + lane * 16;
#pragma unroll
  for (int i = WIN + NQ; i < I_QK; ++i) {
    WAITVM(WIN - 1);
    issue(i);
    if (i == I_G && w == 7) {
#pragma unroll
      for (int j = 0; j < NQP; ++j) G[j] = ld_sc1(rw, goff + j * 1024);
      SB();
    }
    if (i == I_GC && w == 7) {
      u32 bad = 0u;
#pragma unroll
      for (int j = 0; j < NQP; ++j) bad |= gbad(G[j], tag);
      if (!none(bad)) {
        for (u32 spin = 0; spin < SPIN_CAP; ++spin) {
#pragma unroll
          for (int j = 0; j < NQP; ++j) G[j] = ld_sc1(rw, goff + j * 1024);
          bad = 0u;
#pragma unroll
          for (int j = 0; j < NQP; ++j) bad |= gbad(G[j], tag);
          if (none(bad)) break;
          __builtin_amdgcn_s_sleep(4);
        }
      }
      float q0 = gv(G[0].x), q1 = gv(G[0].z);
#pragma unroll
      for (int j = 1; j < NQP; ++j) { q0 += gv(G[j].x); q1 += gv(G[j].z); }
      ((LAS u32*)lds)[L_Q + lane] = cvtpk(q0, q1);
      asm volatile("s_waitcnt lgkmcnt(0)" ::: "memory");
      if (lane == 0) ((volatile LAS u32*)lds)[L_FLAG] = 1u;
      STAMP(2);
      SB();
    }
  }
  STAMP(3);
  while (((volatile LAS u32*)lds)[L_FLAG] == 0u) __builtin_amdgcn_s_sleep(1);
  asm volatile("" ::: "memory");
  const u32x4 q = *(const u32x4*)(lds + L_Q + 4 * c);
  STAMP(4);

  float x[NU];
  constexpr int QPS = 4;
  constexpr int NQS = (NU + QPS - 1) / QPS;
#pragma unroll
  for (int s = 0; s < NQS; ++s) {
    if (I_QK + s < I_END) { WAITVM(WIN - 1); issue(I_QK + s); }
#pragma unroll
    for (int t = s * QPS; t < (s + 1) * QPS && t < NU; ++t)
      x[t] = dot2(K[t].w, q.w, dot2(K[t].z, q.z, dot2(K[t].y, q.y, dot2(K[t].x, q.x, 0.f))));
    SB();
  }
#pragma unroll
  for (int i = I_QK + NQS; i < I_END; ++i) { WAITVM(WIN - 1); issue(i); }
  const bool b3 = c & 8, b2 = c & 4, b1 = c & 2, b0 = c & 1;
  float s0, s1 = -INFINITY;
  {
    float y[8];
#pragma unroll
    for (int i = 0; i < 8; ++i) y[i] = (b3 ? x[i + 8] : x[i]) + dpp<XOR8>(b3 ? x[i] : x[i + 8]);
#pragma unroll
    for (int i = 0; i < 4; ++i) y[i] = (b2 ? y[i + 4] : y[i]) + dpp<XOR7>(b2 ? y[i] : y[i + 4]);
#pragma unroll
    for (int i = 0; i < 2; ++i) y[i] = (b1 ? y[i + 2] : y[i]) + dpp<XOR2>(b1 ? y[i] : y[i + 2]);
    s0 = (b0 ? y[1] : y[0]) + dpp<XOR1>(b0 ? y[0] : y[1]);
  }
  if constexpr (NU > 16) {
    float y[4];
#pragma unroll
    for (int i = 0; i < 2; ++i) y[i] = (b3 ? x[20 + i] : x[16 + i]) + dpp<XOR8>(b3 ? x[16 + i] : x[20 + i]);
#pragma unroll
    for (int i = 2; i < 4; ++i) y[i] = x[16 + i] + dpp<XOR8>(x[16 + i]);
#pragma unroll
    for (int i = 0; i < 2; ++i) y[i] = (b2 ? y[i + 2] : y[i]) + dpp<XOR7>(b2 ? y[i] : y[i + 2]);
    const float z = (b1 ? y[1] : y[0]) + dpp<XOR2>(b1 ? y[0] : y[1]);
    s1 = z + dpp<XOR1>(z);
  }
  {
    const u32 r = cvtpk(s0, s1);
    const u32 r2 = cvtpk(asf(r << 16) * 0.08838834764831845f, asf(r & 0xffff0000u) * 0.08838834764831845f);
    s0 = asf(r2 << 16); s1 = asf(r2 & 0xffff0000u);
    if constexpr (NU > 16) { if ((16 + (c >> 1)) * 8 + w >= nu) s1 = -INFINITY; } else s1 = -INFINITY;
  }
  float m = fmaxf(s0, s1);
  m = fmaxf(m, dpp<XOR1>(m)); m = fmaxf(m, dpp<XOR2>(m)); m = fmaxf(m, dpp<XOR7>(m)); m = fmaxf(m, dpp<XOR8>(m));
  m = x32_max(x16_max(m));
  if (lane == 0) lds[L_M + w] = m;
  asm volatile("s_waitcnt lgkmcnt(0)\n\ts_barrier" ::: "memory");
  STAMP(5);
  {
    const f32x4 a = *(const f32x4*)(lds + L_M), b = *(const f32x4*)(lds + L_M + 4);
    m = fmaxf(fmaxf(fmaxf(a.x, a.y), fmaxf(a.z, a.w)), fmaxf(fmaxf(b.x, b.y), fmaxf(b.z, b.w)));
  }
  constexpr float LOG2E = 1.4426950408889634f;
  const float p0 = __builtin_amdgcn_exp2f((s0 - m) * LOG2E), p1 = __builtin_amdgcn_exp2f((s1 - m) * LOG2E);
  const u32 P0 = cvtpk(p0 - asf(cvtpk(p0, p0) & 0xffff0000u), p0);
  const u32 P1 = cvtpk(p1 - asf(cvtpk(p1, p1) & 0xffff0000u), p1);
  {
    float l = p0 + (b0 ? 0.f : p1);
    l += dpp<XOR1>(l); l += dpp<XOR2>(l); l += dpp<XOR7>(l); l += dpp<XOR8>(l);
    l = x32_sum(x16_sum(l));
    if (lane == 0) lds[L_L + w] = l;
  }
  acc = f32x4{0.f, 0.f, 0.f, 0.f};
#define PV(t) if constexpr ((t) < NU) { const u32 X = (t) < 16 ? dppu<BCAST + ((t) & 15)>(P0) : dppu<BCAST + ((2 * (t)) & 15)>(P1); acc = mfma(afrag(sel, X), V[t], acc); }
  PV(0) PV(1) PV(2) PV(3) PV(4) PV(5) PV(6) PV(7) PV(8) PV(9) PV(10) PV(11) PV(12) PV(13) PV(14) PV(15) PV(16) PV(17) PV(18) PV(19) PV(20) PV(21)
#undef PV
  STAMP(6);
  {
    auto a = __builtin_amdgcn_permlane32_swap(asu(acc.x), asu(acc.z), false, false);
    auto b = __builtin_amdgcn_permlane32_swap(asu(acc.y), asu(acc.w), false, false);
    f32x2 r = {asf(a[0]) + asf(a[1]), asf(b[0]) + asf(b[1])};
    *(f32x2*)(lds + L_RED + w * RSTR + 8 * c + 4 * (g & 1) + 2 * (g >> 1)) = r;
  }
  asm volatile("s_waitcnt lgkmcnt(0)\n\ts_barrier" ::: "memory");
  {
    const f32x2 v = wg_reduce(lds + L_RED, w, lane);
    float l = lds[L_L + (lane & 7)];
    l += dpp<XOR1>(l); l += dpp<XOR2>(l); l += dpp<XOR7>(l);
    const int slot = (br * 32 + h) * 8 + split;
    if ((lane & 7) == 0) st_sc1(rw, WS_P + slot * 1024 + (16 * w + 2 * (lane >> 3)) * 8, gpair(v.x, v.y, tag));
    if (threadIdx.x == 0) st_sc1(rw, WS_ML + slot * 16, gpair(m, l, tag));
  }
  STAMP(7);
  u32 Y;
  {
    const int ns = tb ? 6 : 2, s = c >> 1;
    const bool live = s < ns;
    const int slot = (tb * 32 + h) * 8 + s;
    const int aoff = live ? WS_P + slot * 1024 + (16 * w + 4 * g + 2 * (c & 1)) * 8 : OOB;
    const int moff = live ? WS_ML + slot * 16 : OOB;
    u32x4 a, ml;
    for (u32 spin = 0;; ++spin) {
      a = ld_sc1(rw, aoff); ml = ld_sc1(rw, moff);
      const u32 bad = live ? (gbad(a, tag) | gbad(ml, tag)) : 0u;
      if (none(bad) || spin > SPIN_CAP) break;
    }
    const float ms = live ? gv(ml.x) : -INFINITY;
    float M = ms;
    M = fmaxf(M, dpp<XOR2>(M)); M = fmaxf(M, dpp<XOR8>(M)); M = fmaxf(M, dpp<0x124>(M));
    const float wt = __builtin_amdgcn_exp2f((ms - M) * LOG2E);
    float L = wt * gv(ml.z), n0 = wt * gv(a.x), n1 = wt * gv(a.z);
    L += dpp<XOR2>(L); n0 += dpp<XOR2>(n0); n1 += dpp<XOR2>(n1);
    L += dpp<XOR8>(L); n0 += dpp<XOR8>(n0); n1 += dpp<XOR8>(n1);
    L += dpp<0x124>(L); n0 += dpp<0x124>(n0); n1 += dpp<0x124>(n1);
    const float inv = __builtin_amdgcn_rcpf(L);
    Y = cvtpk(n0 * inv, n1 * inv);
  }
  STAMP(8);
  {
    const u32 Y0 = dppu<0xA0>(Y), Y1 = dppu<0xF5>(Y);
    const u32x4 a0 = afrag(sel, Y0 << 16), a1 = afrag(sel, Y0), a2 = afrag(sel, Y1 << 16), a3 = afrag(sel, Y1);
#pragma unroll
    for (int k = 0; k < NT; ++k) {
      f32x4 d = {0.f, 0.f, 0.f, 0.f};
      d = mfma(a0, WO[4 * k + 0], d); d = mfma(a1, WO[4 * k + 1], d); d = mfma(a2, WO[4 * k + 2], d); d = mfma(a3, WO[4 * k + 3], d);
      if (lane < 32) *(f32x4*)(lds + L_RED + ((k + 1) * NWAVE + w) * RSTR + 8 * c + 4 * g) = d;
    }
  }
  asm volatile("s_waitcnt lgkmcnt(0)\n\ts_barrier" ::: "memory");
#pragma unroll
  for (int k = 0; k < NT; ++k) {
    const f32x2 v = wg_reduce(lds + L_RED + (k + 1) * NWAVE * RSTR, w, lane);
    const int pair = tb * 320 + (tk + k) * 64 + 8 * w + (lane >> 3);
    if ((lane & 7) == 0) st_sc1(rw, WS_Y + (pair * 32 + h) * 16, gpair(v.x, v.y, tag));
  }
  STAMP(9);
  const int b = blockIdx.x;
  if (w == 0 || (w == 1 && b < 64)) {
    const int quad = w == 0 ? b : 256 + b;
    const bool closer = (w == 1 && b == 0);
    u32x4 y;
    for (u32 spin = 0;; ++spin) {
      y = ld_sc1(rw, WS_Y + quad * 1024 + lane * 16);
      const u32x4 z = ld_sc1(rw, closer ? WS_Y + lane * 16 : OOB);
      const u32 bad = gbad(y, tag) | (closer ? gbad(z, tag) : 0u);
      if (none(bad) || spin > SPIN_CAP) break;
    }
    float o0 = gv(y.x), o1 = gv(y.z);
    o0 += dpp<XOR1>(o0); o1 += dpp<XOR1>(o1);
    o0 += dpp<XOR2>(o0); o1 += dpp<XOR2>(o1);
    o0 += dpp<XOR7>(o0); o1 += dpp<XOR7>(o1);
    o0 += dpp<XOR8>(o0); o1 += dpp<XOR8>(o1);
    o0 = x16_sum(o0); o1 = x16_sum(o1);
    if ((lane & 31) == 0) p.out[quad * 2 + (lane >> 5)] = cvtpk(o0, o1);
    if (closer && lane == 0) *(u64*)((char*)p.ws + WS_EPOCH) = ep;
    STAMP(10);
  }
}

__global__ void __launch_bounds__(NTHR, 2) fused_decode(Params p) {
  __shared__ __attribute__((aligned(16))) float lds[L_TOT];
  const int b = blockIdx.x, slot = b >> 3, role = slot >> 2, h = (b & 7) + 8 * (slot & 3);
  const int lane = threadIdx.x & 63, w = __builtin_amdgcn_readfirstlane(threadIdx.x >> 6);
  if (role >= 6) body<true>(p, lds, role, h, w, lane);
  else body<false>(p, lds, role, h, w, lane);
}

extern "C" void kernel_launch(void* const* d_in, const int* in_sizes, int n_in, void* d_out, int out_size, void* d_ws, size_t ws_size,
                              hipStream_t stream) {
  Params p;
  p.xA = (const u16*)d_in[0]; p.kA = (const u16*)d_in[1]; p.vA = (const u16*)d_in[2]; p.wqA = (const u16*)d_in[3]; p.woA = (const u16*)d_in[4];
  p.xB = (const u16*)d_in[5]; p.kB = (const u16*)d_in[6]; p.vB = (const u16*)d_in[7]; p.wqB = (const u16*)d_in[8]; p.woB = (const u16*)d_in[9];
  p.out = (u32*)d_out; p.ws = d_ws;
  fused_decode<<<256, NTHR, 0, stream>>>(p);
}
```

```cpp
#include <hip/hip_runtime.h>
#include <hip/hip_bf16.h>
#include <stdint.h>

typedef unsigned int u32;
typedef unsigned short u16;
typedef unsigned long long u64;
typedef u32 u32x2 __attribute__((ext_vector_type(2)));
typedef u32 u32x4 __attribute__((ext_vector_type(4)));
typedef float f32x2 __attribute__((ext_vector_type(2)));
typedef float f32x4 __attribute__((ext_vector_type(4)));
typedef __bf16 bf16x2 __attribute__((ext_vector_type(2)));
typedef __bf16 bf16x8 __attribute__((ext_vector_type(8)));
#define LAS __attribute__((address_space(3)))
#define GAS __attribute__((address_space(1)))

constexpr int NWAVE = 8, NTHR = NWAVE * 64;
constexpr int WIN_EVEN = 4, WIN_ODD = 5;
constexpr int STG = 4;
constexpr int OOB = 0x7fffff00;
constexpr int AUX_NT = 2, AUX_SC1 = 16;
constexpr u32 SPIN_CAP = 200000;
constexpr float LOG2E = 1.4426950408889634f;

constexpr int WS_EPOCH = 0;
constexpr int WS_Q = 4096;
constexpr int WS_P = WS_Q + 64 * 6 * 1024;
constexpr int WS_ML = WS_P + 64 * 8 * 1024;
constexpr int WS_Y = WS_ML + 64 * 8 * 128;
constexpr int WS_END = WS_Y + 64 * 640 * 8;

struct Params {
  const u16 *xA, *kA, *vA, *wqA, *woA, *xB, *kB, *vB, *wqB, *woB;
  u32* out;
  void* ws;
};

#define RSRC(ptr, n) __builtin_amdgcn_make_buffer_rsrc((void*)(ptr), 0, (n), 0x00020000)
#define SB() __builtin_amdgcn_sched_barrier(0)
#define WAITVM(N) __builtin_amdgcn_s_waitcnt(((N) & 15) | (7 << 4) | (15 << 8) | (((N) >> 4) << 14))

__device__ __forceinline__ u32x4 ld_nt(__amdgpu_buffer_rsrc_t r, int off, int soff = 0) { return __builtin_amdgcn_raw_buffer_load_b128(r, off, soff, AUX_NT); }
__device__ __forceinline__ u32x4 ld_sc1(__amdgpu_buffer_rsrc_t r, int off) {
  asm volatile("" ::: "memory");
  return __builtin_amdgcn_raw_buffer_load_b128(r, off, 0, AUX_SC1);
}
__device__ __forceinline__ void st_sc1(__amdgpu_buffer_rsrc_t r, int off, u32x4 v) { __builtin_amdgcn_raw_buffer_store_b128(v, r, off, 0, AUX_SC1); }
__device__ __forceinline__ float asf(u32 x) { return __builtin_bit_cast(float, x); }
__device__ __forceinline__ u32 asu(float x) { return __builtin_bit_cast(u32, x); }
__device__ __forceinline__ u32 cvtpk(float lo, float hi) {
  f32x2 v = {lo, hi};
  return __builtin_bit_cast(u32, __builtin_convertvector(v, bf16x2));
}
__device__ __forceinline__ float dot2(u32 a, u32 b, float c) {
  return __builtin_amdgcn_fdot2_f32_bf16(__builtin_bit_cast(bf16x2, a), __builtin_bit_cast(bf16x2, b), c, false);
}
template <int CTRL> __device__ __forceinline__ float dpp(float x) { return asf((u32)__builtin_amdgcn_mov_dpp((int)asu(x), CTRL, 0xf, 0xf, true)); }
template <int CTRL> __device__ __forceinline__ u32 dppu(u32 x) { return (u32)__builtin_amdgcn_mov_dpp((int)x, CTRL, 0xf, 0xf, true); }
constexpr int XOR1 = 0xB1, XOR2 = 0x4E, XOR7 = 0x141, XOR8 = 0x128, ROR4 = 0x124, BCAST = 0x150;
__device__ __forceinline__ float x16_sum(float x) { auto s = __builtin_amdgcn_permlane16_swap(asu(x), asu(x), false, false); return asf(s[0]) + asf(s[1]); }
__device__ __forceinline__ float x32_sum(float x) { auto s = __builtin_amdgcn_permlane32_swap(asu(x), asu(x), false, false); return asf(s[0]) + asf(s[1]); }
__device__ __forceinline__ float x16_max(float x) { auto s = __builtin_amdgcn_permlane16_swap(asu(x), asu(x), false, false); return fmaxf(asf(s[0]), asf(s[1])); }
__device__ __forceinline__ float x32_max(float x) { auto s = __builtin_amdgcn_permlane32_swap(asu(x), asu(x), false, false); return fmaxf(asf(s[0]), asf(s[1])); }
__device__ __forceinline__ f32x4 mfma(u32x4 a, u32x4 b, f32x4 c) {
  return __builtin_amdgcn_mfma_f32_16x16x32_bf16(__builtin_bit_cast(bf16x8, a), __builtin_bit_cast(bf16x8, b), c, 0, 0, 0);
}
struct Sel { u32 s[4]; };
__device__ __forceinline__ u32x4 afrag(const Sel& sl, u32 X) {
  u32x4 a;
  a.x = __builtin_amdgcn_perm(X, X, sl.s[0]); a.y = __builtin_amdgcn_perm(X, X, sl.s[1]);
  a.z = __builtin_amdgcn_perm(X, X, sl.s[2]); a.w = __builtin_amdgcn_perm(X, X, sl.s[3]);
  return a;
}
struct Tag { u32 lo8; u32 hi; };
__device__ __forceinline__ u32 gpack(float v, const Tag& t) { return __builtin_amdgcn_alignbyte(t.lo8, asu(v) + 0x80u, 1); }
__device__ __forceinline__ u32x4 gpair(float a, float b, const Tag& t) { u32x4 g = {gpack(a, t), t.hi, gpack(b, t), t.hi}; return g; }
__device__ __forceinline__ u32 gbad(u32x4 g, const Tag& t) {
  return (g.y ^ t.hi) | (g.w ^ t.hi) | ((g.x >> 24) ^ t.lo8) | ((g.z >> 24) ^ t.lo8);
}
__device__ __forceinline__ bool none(u32 bad) { return __builtin_amdgcn_ballot_w64(bad != 0u) == 0ull; }
__device__ __forceinline__ float gv(u32 x) { return asf(x << 8); }

constexpr int RSTR = 136;
constexpr int L_RED = 0;
constexpr int L_Q = 3 * NWAVE * RSTR;
constexpr int L_M = L_Q + 6 * 128;
constexpr int L_L = L_M + 8;
constexpr int L_CTL = L_L + 8;
constexpr int L_TOT = L_CTL + 4;

__device__ __forceinline__ f32x2 wg_reduce(const float* red, int w, int lane) {
  f32x2 v = *(const f32x2*)(red + (lane & 7) * RSTR + 16 * w + 2 * (lane >> 3));
  v.x += dpp<XOR1>(v.x); v.y += dpp<XOR1>(v.y);
  v.x += dpp<XOR2>(v.x); v.y += dpp<XOR2>(v.y);
  v.x += dpp<XOR7>(v.x); v.y += dpp<XOR7>(v.y);
  return v;
}

__device__ __forceinline__ int kv_off(int j, int w) { return (j >> 2) * 32768 + ((w + (j >> 2)) & 7) * 4096 + (j & 3) * 1024; }

struct Pre { u32x2 xa, xb, ep; u32x4 q0, q1, k0, k1; };

template <bool ISA, int WIN>
__device__ __forceinline__ void body(const u16* kp, const u16* vp, const u16* wqp, const u16* wop, u32* out, void* ws, float* lds,
                                     const int role, const int h, const int w, const int lane, const Pre& pre) {
  constexpr int NT = ISA ? 2 : 1;
  constexpr int NO = 4 * NT;
  constexpr int NQ = 4 * NT;
  constexpr int NU = ISA ? 16 : 22;
  constexpr int NBLK = ISA ? 32 : 128;
  constexpr int br = ISA ? 0 : 1;
  const int g = lane >> 4, c = lane & 15;
  const int tb = ISA ? 0 : (role < 5 ? 1 : 0);
  const int tk = ISA ? (role - 6) * 2 : (role < 5 ? role : 4);
  const int split = ISA ? role - 6 : role;
  const int qslot = ISA ? split : (role < 5 ? role : 2);

  const auto rq = RSRC(wqp + (size_t)tk * (128 * 4096) + h * 128, NQ * 32 * 8192);
  const auto ro = RSRC(wop + (size_t)h * (128 * 640) + tk * 128, 128 * 1280);
  const size_t kvoff = (size_t)h * (4096 * 128) + (ISA ? 3072 * 128 : 0);
  const auto rk = RSRC(kp + kvoff, NBLK * 8192);
  const auto rv = RSRC(vp + kvoff, NBLK * 8192);
  const auto rw = RSRC(ws, WS_END);

  const u32 xd[4] = {pre.xa.x, pre.xa.y, pre.xb.x, pre.xb.y};
  u32x4 WQ[NQ], WO[NO], K[NU], V[NU];
  const int qoff = (w * 4 + g) * NQ * 8192 + c * 16;
  const int ooff = (w * 16 + g * 4) * 1280 + c * 16;
  const int koff = lane * 16;
  const int blk0 = split * (ISA ? 16 : 21) + h * STG;
  const int xe = w * 6 + split;
  const bool xlive = !ISA && xe < 16;
  const int xso = kv_off((126 + (xe >> 3) + h * STG) & 127, xe & 7), xvo = xlive ? koff : OOB;
  constexpr int I_K = NQ + 2, I_V = NQ + NU, I_END = I_V + NU;
  WQ[0] = pre.q0; WQ[1] = pre.q1; K[0] = pre.k0; K[1] = pre.k1;
  auto issue = [&](int i) {
    if (i < I_K) WQ[i - 2] = ld_nt(rq, qoff + (i - 2) * 8192);
    else if (!ISA && i == I_V - 1) K[NU - 1] = ld_nt(rk, xvo, xso);
    else if (!ISA && i == I_END - 1) V[NU - 1] = ld_nt(rv, xvo, xso);
    else if (i < I_V) K[i - NQ] = ld_nt(rk, koff, kv_off((blk0 + i - NQ) & (NBLK - 1), w));
    else if (i < I_END) V[i - I_V] = ld_nt(rv, koff, kv_off((blk0 + i - I_V) & (NBLK - 1), w));
    SB();
  };
  auto step = [&](int i) { if (i < I_END) { WAITVM(WIN - 1); issue(i); } };
#pragma unroll
  for (int i = 4; i < WIN; ++i) issue(i);
  u32 pt = 0;
  if (w < 3) {
    const char* a = w == 0 ? (const char*)(vp + kvoff) : w == 1 ? (const char*)(wop + (size_t)h * (128 * 640)) : (const char*)out;
    const u32 o = w == 1 ? (lane & 1) * (128 * 1280 - 64) : 0u;
    if (lane < 2) pt = *(const GAS u32*)(a + o);
  }
  SB();
  Sel sel;
  {
    const u32 src = (c & 8) ? 0x0100u : 0x0302u;
    const u32 m = (c & 1) ? ((src << 16) | 0x0c0cu) : (0x0c0c0000u | src);
#pragma unroll
    for (int i = 0; i < 4; ++i) sel.s[i] = (((c & 7) >> 1) == i) ? m : 0x0c0c0c0cu;
  }
  volatile LAS u32* ctl = (volatile LAS u32*)lds + L_CTL;
  if (threadIdx.x < 4) ctl[threadIdx.x] = 0u;
  asm volatile("s_waitcnt lgkmcnt(0)\n\ts_barrier" ::: "memory");
  SB();

  f32x4 acc = {0.f, 0.f, 0.f, 0.f};
  constexpr int I_S = I_K + WIN;
#pragma unroll
  for (int i = 0; i < I_K; ++i) {
    WAITVM(WIN - 1);
    if (i < 2 || i >= 4) {
      const int t = i < 2 ? i : i - 2;
      const u32 X = (t & 1) ? xd[t >> 1] : (xd[t >> 1] << 16);
      acc = mfma(afrag(sel, X), WQ[t], acc);
    }
    SB();
    issue(i + WIN);
  }
  if (lane < 32) *(f32x4*)(lds + L_RED + w * RSTR + 8 * c + 4 * g) = acc;
  asm volatile("s_waitcnt lgkmcnt(0)" ::: "memory");
  if (lane == 0) __hip_atomic_fetch_add((LAS u32*)ctl + 1, 1u, __ATOMIC_RELAXED, __HIP_MEMORY_SCOPE_WORKGROUP);
  SB();
  const u64 ep = (((u64)__builtin_amdgcn_readfirstlane(pre.ep.y) << 32) | (u32)__builtin_amdgcn_readfirstlane(pre.ep.x)) + 1ull;
  Tag tag; tag.lo8 = (u32)ep & 0xffu; tag.hi = (u32)(ep >> 8);
  SB();

  constexpr int NQP = ISA ? 3 : 5;
  constexpr int I_P = I_S + 2;
  constexpr int I_G = I_V - (ISA ? 2 : 6);
  constexpr int I_GC = I_G + WIN;
  constexpr int I_QK = I_V + (ISA ? 7 : WIN);
  static_assert(I_P < I_G && I_GC < I_QK && I_QK >= I_V + WIN, "order");
  u32x4 G;
  const int goff = WS_Q + ((br * 32 + h) * 6 + w) * 1024 + lane * 16;
#pragma unroll
  for (int i = I_S; i < I_QK; ++i) {
    step(i);
    if (i == I_P) {
      while (ctl[1] != (u32)NWAVE) __builtin_amdgcn_s_sleep(1);
      asm volatile("" ::: "memory");
      const f32x2 v = wg_reduce(lds + L_RED, w, lane);
      if ((lane & 7) == 0) st_sc1(rw, WS_Q + ((tb * 32 + h) * 6 + qslot) * 1024 + (16 * w + 2 * (lane >> 3)) * 8, gpair(v.x, v.y, tag));
      asm volatile("" :: "v"(pt));
      SB();
    }
    if (i == I_G && w < NQP) { G = ld_sc1(rw, goff); SB(); }
    if (i == I_GC && w < NQP) {
      if (!none(gbad(G, tag))) {
        for (u32 spin = 0; spin < SPIN_CAP; ++spin) {
          G = ld_sc1(rw, goff);
          if (none(gbad(G, tag))) break;
          __builtin_amdgcn_s_sleep(4);
        }
      }
      *(f32x2*)(lds + L_Q + w * 128 + 2 * lane) = f32x2{gv(G.x), gv(G.z)};
      asm volatile("s_waitcnt lgkmcnt(0)" ::: "memory");
      if (lane == 0) __hip_atomic_fetch_add((LAS u32*)ctl, 1u, __ATOMIC_RELAXED, __HIP_MEMORY_SCOPE_WORKGROUP);
      SB();
    }
  }
  while (ctl[0] != (u32)NQP) __builtin_amdgcn_s_sleep(1);
  asm volatile("" ::: "memory");
  u32x4 q;
  {
    f32x4 qa = *(const f32x4*)(lds + L_Q + 8 * c), qb = *(const f32x4*)(lds + L_Q + 8 * c + 4);
#pragma unroll
    for (int j = 1; j < NQP; ++j) { qa += *(const f32x4*)(lds + L_Q + j * 128 + 8 * c); qb += *(const f32x4*)(lds + L_Q + j * 128 + 8 * c + 4); }
    q.x = cvtpk(qa.x, qa.y); q.y = cvtpk(qa.z, qa.w); q.z = cvtpk(qb.x, qb.y); q.w = cvtpk(qb.z, qb.w);
  }

  float x[NU];
  constexpr int QPS = 4;
  constexpr int NQS = (NU + QPS - 1) / QPS;
#pragma unroll
  for (int s = 0; s < NQS; ++s) {
    step(I_QK + s);
#pragma unroll
    for (int t = s * QPS; t < (s + 1) * QPS && t < NU; ++t)
      x[t] = dot2(K[t].w, q.w, dot2(K[t].z, q.z, dot2(K[t].y, q.y, dot2(K[t].x, q.x, 0.f))));
    SB();
  }
  constexpr int I_R = I_QK + NQS;
  step(I_R);
  const bool b3 = c & 8, b2 = c & 4, b1 = c & 2, b0 = c & 1;
  float s0, s1 = -INFINITY;
  {
    float y[8];
#pragma unroll
    for (int i = 0; i < 8; ++i) y[i] = (b3 ? x[i + 8] : x[i]) + dpp<XOR8>(b3 ? x[i] : x[i + 8]);
#pragma unroll
    for (int i = 0; i < 4; ++i) y[i] = (b2 ? y[i + 4] : y[i]) + dpp<XOR7>(b2 ? y[i] : y[i + 4]);
#pragma unroll
    for (int i = 0; i < 2; ++i) y[i] = (b1 ? y[i + 2] : y[i]) + dpp<XOR2>(b1 ? y[i] : y[i + 2]);
    s0 = (b0 ? y[1] : y[0]) + dpp<XOR1>(b0 ? y[0] : y[1]);
  }
  SB();
  step(I_R + 1);
  if constexpr (NU > 16) {
    float y[4];
#pragma unroll
    for (int i = 0; i < 4; ++i) {
      if (20 + i < NU) y[i] = (b3 ? x[20 + i] : x[16 + i]) + dpp<XOR8>(b3 ? x[16 + i] : x[20 + i]);
      else y[i] = x[16 + i] + dpp<XOR8>(x[16 + i]);
    }
#pragma unroll
    for (int i = 0; i < 2; ++i) y[i] = (b2 ? y[i + 2] : y[i]) + dpp<XOR7>(b2 ? y[i] : y[i + 2]);
    const float z = (b1 ? y[1] : y[0]) + dpp<XOR2>(b1 ? y[0] : y[1]);
    s1 = z + dpp<XOR1>(z);
  }
  {
    const u32 r = cvtpk(s0, s1);
    const u32 r2 = cvtpk(asf(r << 16) * 0.08838834764831845f, asf(r & 0xffff0000u) * 0.08838834764831845f);
    s0 = asf(r2 << 16); s1 = asf(r2 & 0xffff0000u);
    if ((c >> 1) >= (xlive ? 6 : ISA ? 0 : 5)) s1 = -INFINITY;
  }
  float m = fmaxf(s0, s1);
  m = fmaxf(m, dpp<XOR1>(m)); m = fmaxf(m, dpp<XOR2>(m)); m = fmaxf(m, dpp<XOR7>(m)); m = fmaxf(m, dpp<XOR8>(m));
  m = x32_max(x16_max(m));
  if (lane == 0) {
    lds[L_M + w] = m;
    asm volatile("s_waitcnt lgkmcnt(0)" ::: "memory");
    __hip_atomic_fetch_add((LAS u32*)ctl + 2, 1u, __ATOMIC_RELAXED, __HIP_MEMORY_SCOPE_WORKGROUP);
  }
  const float p0 = __builtin_amdgcn_exp2f((s0 - m) * LOG2E), p1 = __builtin_amdgcn_exp2f((s1 - m) * LOG2E);
  const u32 P0 = cvtpk(p0 - asf(cvtpk(p0, p0) & 0xffff0000u), p0);
  const u32 P1 = cvtpk(p1 - asf(cvtpk(p1, p1) & 0xffff0000u), p1);
  float l = p0 + (b0 ? 0.f : p1);
  l += dpp<XOR1>(l); l += dpp<XOR2>(l); l += dpp<XOR7>(l); l += dpp<XOR8>(l);
  l = x32_sum(x16_sum(l));
  SB();
  const bool live = (c >> 1) < (tb ? 6 : 2);
  const int aoff = WS_P + ((tb * 32 + h) * 8 + (c >> 1)) * 1024 + (16 * w + 4 * g + 2 * (c & 1)) * 8;
  const int moff = WS_ML + ((tb * 32 + h) * 8 + (c >> 1)) * 128;
  const int b = blockIdx.x;
  const bool red = (w == 0 || (w == 1 && b < 64));
  const int quad = w == 0 ? b : 256 + b;
  const int lo = (lane >> 1) * (640 * 8) + (lane & 1) * 16;
  const int yoff = WS_Y + (quad >= 160 ? 32 * 640 * 8 + (quad - 160) * 32 : quad * 32) + lo;
  u32x4 a = ld_sc1(rw, live ? aoff : OOB), ml = ld_sc1(rw, live ? moff : OOB), y = {0u, 0u, 0u, 0u};
  if (red) y = ld_sc1(rw, yoff);
  SB();

  acc = f32x4{0.f, 0.f, 0.f, 0.f};
  constexpr int I_PV = I_R + 2, NPS = I_END - I_PV;
  constexpr int NEARLY = NU - WIN;
  static_assert(NPS >= 0 && NPS <= NEARLY, "every remaining load gets its step");
  float M = 0.f, sc = 0.f;
#define PV(t) if constexpr ((t) < NU) { \
    if constexpr ((t) < NEARLY && ((t) * NPS) % NEARLY < NPS) { SB(); step(I_PV + ((t) * NPS) / NEARLY); } \
    if constexpr ((t) == NEARLY) { \
      SB(); \
      while (ctl[2] != (u32)NWAVE) __builtin_amdgcn_s_sleep(1); \
      asm volatile("" ::: "memory"); \
      const f32x4 ma = *(const f32x4*)(lds + L_M), mb = *(const f32x4*)(lds + L_M + 4); \
      M = fmaxf(fmaxf(fmaxf(ma.x, ma.y), fmaxf(ma.z, ma.w)), fmaxf(fmaxf(mb.x, mb.y), fmaxf(mb.z, mb.w))); \
      sc = __builtin_amdgcn_exp2f((m - M) * LOG2E); \
      if (lane == 0) lds[L_L + w] = l * sc; \
      SB(); \
    } \
    const u32 X = (t) < 16 ? dppu<BCAST + ((t) & 15)>(P0) : dppu<BCAST + ((2 * (t)) & 15)>(P1); \
    acc = mfma(afrag(sel, X), V[t], acc); \
    if constexpr ((t) == NU - 2) { \
      SB(); \
      _Pragma("unroll") for (int o = 0; o < NO; ++o) WO[o] = ld_nt(ro, ooff + (o & 3) * 1280 + (o >> 2) * 256); \
      SB(); \
    } }
  PV(0) PV(1) PV(2) PV(3) PV(4) PV(5) PV(6) PV(7) PV(8) PV(9) PV(10) PV(11) PV(12) PV(13) PV(14) PV(15) PV(16) PV(17) PV(18) PV(19) PV(20) PV(21)
#undef PV
  {
    auto u = __builtin_amdgcn_permlane32_swap(asu(acc.x), asu(acc.z), false, false);
    auto v = __builtin_amdgcn_permlane32_swap(asu(acc.y), asu(acc.w), false, false);
    const f32x2 r = {(asf(u[0]) + asf(u[1])) * sc, (asf(v[0]) + asf(v[1])) * sc};
    *(f32x2*)(lds + L_RED + w * RSTR + 8 * c + 4 * (g & 1) + 2 * (g >> 1)) = r;
  }
  asm volatile("s_waitcnt lgkmcnt(0)\n\ts_barrier" ::: "memory");
  {
    const f32x2 v = wg_reduce(lds + L_RED, w, lane);
    float L = lds[L_L + (lane & 7)];
    L += dpp<XOR1>(L); L += dpp<XOR2>(L); L += dpp<XOR7>(L);
    const int slot = (br * 32 + h) * 8 + split;
    if ((lane & 7) == 0) st_sc1(rw, WS_P + slot * 1024 + (16 * w + 2 * (lane >> 3)) * 8, gpair(v.x, v.y, tag));
    if (threadIdx.x < 8) st_sc1(rw, WS_ML + slot * 128 + lane * 16, gpair(M, L, tag));
  }

  u32 Y;
  {
    bool na = live && gbad(a, tag) != 0u, nm = live && gbad(ml, tag) != 0u;
    for (u32 spin = 0; spin < SPIN_CAP; ++spin) {
      if (na) a = ld_sc1(rw, aoff);
      if (nm) ml = ld_sc1(rw, moff);
      na = live && gbad(a, tag) != 0u; nm = live && gbad(ml, tag) != 0u;
      if (__builtin_amdgcn_ballot_w64(na || nm) == 0ull) break;
    }
    const float ms = live ? gv(ml.x) : -INFINITY;
    float Mx = ms;
    Mx = fmaxf(Mx, dpp<XOR2>(Mx)); Mx = fmaxf(Mx, dpp<XOR8>(Mx)); Mx = fmaxf(Mx, dpp<ROR4>(Mx));
    const float wt = __builtin_amdgcn_exp2f((ms - Mx) * LOG2E);
    float L = wt * gv(ml.z), n0 = wt * gv(a.x), n1 = wt * gv(a.z);
    L += dpp<XOR2>(L); n0 += dpp<XOR2>(n0); n1 += dpp<XOR2>(n1);
    L += dpp<XOR8>(L); n0 += dpp<XOR8>(n0); n1 += dpp<XOR8>(n1);
    L += dpp<ROR4>(L); n0 += dpp<ROR4>(n0); n1 += dpp<ROR4>(n1);
    const float inv = __builtin_amdgcn_rcpf(L);
    Y = cvtpk(n0 * inv, n1 * inv);
  }
  {
    const u32 Y0 = dppu<0xA0>(Y), Y1 = dppu<0xF5>(Y);
    const u32x4 a0 = afrag(sel, Y0 << 16), a1 = afrag(sel, Y0), a2 = afrag(sel, Y1 << 16), a3 = afrag(sel, Y1);
#pragma unroll
    for (int k = 0; k < NT; ++k) {
      f32x4 d = {0.f, 0.f, 0.f, 0.f};
      d = mfma(a0, WO[4 * k + 0], d); d = mfma(a1, WO[4 * k + 1], d); d = mfma(a2, WO[4 * k + 2], d); d = mfma(a3, WO[4 * k + 3], d);
      if (lane < 32) *(f32x4*)(lds + L_RED + ((k + 1) * NWAVE + w) * RSTR + 8 * c + 4 * g) = d;
    }
  }
  asm volatile("s_waitcnt lgkmcnt(0)\n\ts_barrier" ::: "memory");
#pragma unroll
  for (int k = 0; k < NT; ++k) {
    const f32x2 v = wg_reduce(lds + L_RED + (k + 1) * NWAVE * RSTR, w, lane);
    if ((lane & 7) == 0) st_sc1(rw, WS_Y + ((tb * 32 + h) * 640 + (tk + k) * 128 + 16 * w + 2 * (lane >> 3)) * 8, gpair(v.x, v.y, tag));
  }

  if (red) {
    const bool closer = (w == 1 && b == 0);
    u32x4 z = {0u, 0u, 0u, 0u};
    bool ny = gbad(y, tag) != 0u, nz = closer;
    for (u32 spin = 0; spin < SPIN_CAP; ++spin) {
      if (ny) y = ld_sc1(rw, yoff);
      if (nz) z = ld_sc1(rw, WS_Y + lo);
      ny = gbad(y, tag) != 0u; nz = closer && gbad(z, tag) != 0u;
      if (__builtin_amdgcn_ballot_w64(ny || nz) == 0ull) break;
    }
    float o0 = gv(y.x), o1 = gv(y.z);
    o0 += dpp<XOR2>(o0); o1 += dpp<XOR2>(o1);
    o0 += dpp<XOR8>(o0); o1 += dpp<XOR8>(o1);
    o0 += dpp<ROR4>(o0); o1 += dpp<ROR4>(o1);
    o0 = x32_sum(x16_sum(o0)); o1 = x32_sum(x16_sum(o1));
    if (lane < 2) __hip_atomic_store((GAS u32*)out + quad * 2 + lane, cvtpk(o0, o1), __ATOMIC_RELAXED, __HIP_MEMORY_SCOPE_AGENT);
    if (closer && lane == 0) __hip_atomic_store((GAS u64*)((char*)ws + WS_EPOCH), ep, __ATOMIC_RELAXED, __HIP_MEMORY_SCOPE_AGENT);
  }
}

__global__ void __launch_bounds__(NTHR, 2) fused_decode(Params p) {
  __shared__ __attribute__((aligned(16))) float lds[L_TOT];
  const int b = blockIdx.x, role = b >> 5, h = b & 31;
  const int lane = threadIdx.x & 63, w = __builtin_amdgcn_readfirstlane(threadIdx.x >> 6);
  const bool isA = role >= 6;
  const int nq = isA ? 8 : 4;
  const int qrow0 = ((0x20443210u >> (4 * role)) & 15) * 128;
  const int blk0 = (int)((0x100069543F2A1500ull >> (8 * role)) & 255) + h * STG, msk = isA ? 31 : 127;
  const int r0 = qrow0 + (w * 4 + (lane >> 4)) * nq;
  const u32 kvo = (u32)h * (4096 * 256) + (isA ? 3072 * 256 : 0);
  int xo = r0 * 2, xo2 = r0 * 2 + 8, qo = r0 * 8192 + (lane & 15) * 16, ko = lane * 16;
  int so0 = kvo + kv_off(blk0 & msk, w), so1 = kvo + kv_off((blk0 + 1) & msk, w), sq0 = h * 256, sq1 = h * 256 + 8192, role2 = role;
  asm volatile("" : "+v"(xo), "+v"(xo2), "+v"(qo), "+v"(ko), "+s"(so0), "+s"(so1), "+s"(sq0), "+s"(sq1), "+s"(role2));
  SB();
  const bool isA2 = role2 >= 6, tB2 = role2 < 5;
  const u16* kp = isA2 ? p.kA : p.kB;
  const u16* vp = isA2 ? p.vA : p.vB;
  const u16* wqp = tB2 ? p.wqB : p.wqA;
  const u16* xp = tB2 ? p.xB : p.xA;
  const u16* wop = tB2 ? p.woB : p.woA;
  u32* out = p.out;
  void* ws = p.ws;
  asm volatile("" : "+s"(kp), "+s"(vp), "+s"(wqp), "+s"(wop), "+s"(xp), "+s"(out), "+s"(ws));
  Pre pre;
  {
    const auto rq = RSRC(wqp, 640 * 8192);
    const auto rx = RSRC(xp, 1280);
    const auto rk = RSRC(kp, 32 * 4096 * 256);
    pre.xa = __builtin_amdgcn_raw_buffer_load_b64(rx, xo, 0, 0);
    pre.xb = __builtin_amdgcn_raw_buffer_load_b64(rx, xo2, 0, 0);
    pre.ep = __builtin_amdgcn_raw_buffer_load_b64(RSRC(ws, 64), WS_EPOCH, 0, AUX_SC1);
    SB();
    pre.q0 = ld_nt(rq, qo, sq0);
    pre.q1 = ld_nt(rq, qo, sq1);
    pre.k0 = ld_nt(rk, ko, so0);
    pre.k1 = ld_nt(rk, ko, so1);
    SB();
  }
  u32 xcc;
  asm volatile("s_getreg_b32 %0, hwreg(HW_REG_XCC_ID)" : "=s"(xcc));
  if (xcc & 1) {
    if (isA) body<true, WIN_ODD>(kp, vp, wqp, wop, out, ws, lds, role, h, w, lane, pre);
    else body<false, WIN_ODD>(kp, vp, wqp, wop, out, ws, lds, role, h, w, lane, pre);
  } else {
    if (isA) body<true, WIN_EVEN>(kp, vp, wqp, wop, out, ws, lds, role, h, w, lane, pre);
    else body<false, WIN_EVEN>(kp, vp, wqp, wop, out, ws, lds, role, h, w, lane, pre);
  }
}

extern "C" void kernel_launch(void* const* d_in, const int* in_sizes, int n_in, void* d_out, int out_size, void* d_ws, size_t ws_size,
                              hipStream_t stream) {
  Params p;
  p.xA = (const u16*)d_in[0]; p.kA = (const u16*)d_in[1]; p.vA = (const u16*)d_in[2]; p.wqA = (const u16*)d_in[3]; p.woA = (const u16*)d_in[4];
  p.xB = (const u16*)d_in[5]; p.kB = (const u16*)d_in[6]; p.vB = (const u16*)d_in[7]; p.wqB = (const u16*)d_in[8]; p.woB = (const u16*)d_in[9];
  p.out = (u32*)d_out; p.ws = d_ws;
  fused_decode<<<256, NTHR, 0, stream>>>(p);
}
```

```cpp
#include <hip/hip_runtime.h>
#include <hip/hip_bf16.h>
#include <stdint.h>

typedef unsigned int u32;
typedef unsigned short u16;
typedef unsigned long long u64;
typedef u32 u32x2 __attribute__((ext_vector_type(2)));
typedef u32 u32x4 __attribute__((ext_vector_type(4)));
typedef float f32x2 __attribute__((ext_vector_type(2)));
typedef float f32x4 __attribute__((ext_vector_type(4)));
typedef __bf16 bf16x2 __attribute__((ext_vector_type(2)));
typedef __bf16 bf16x8 __attribute__((ext_vector_type(8)));
#define LAS __attribute__((address_space(3)))
#define GAS __attribute__((address_space(1)))

constexpr int NWAVE = 8, NTHR = NWAVE * 64;
constexpr int WIN = 5;
constexpr int STG = 4;
constexpr int OOB = 0x7fffff00;
constexpr int AUX_NT = 2, AUX_SC1 = 16;
constexpr u32 SPIN_CAP = 200000;
constexpr float LOG2E = 1.4426950408889634f;

constexpr int WS_EPOCH = 0;
constexpr int WS_Q = 4096;
constexpr int WS_P = WS_Q + 64 * 6 * 1024;
constexpr int WS_ML = WS_P + 64 * 8 * 1024;
constexpr int WS_Y = WS_ML + 64 * 8 * 128;
constexpr int WS_END = WS_Y + 64 * 640 * 8;

struct Params {
  const u16 *xA, *kA, *vA, *wqA, *woA, *xB, *kB, *vB, *wqB, *woB;
  u32* out;
  void* ws;
};

#define RSRC(ptr, n) __builtin_amdgcn_make_buffer_rsrc((void*)(ptr), 0, (n), 0x00020000)
#define SB() __builtin_amdgcn_sched_barrier(0)
#define WAITVM(N) __builtin_amdgcn_s_waitcnt(((N) & 15) | (7 << 4) | (15 << 8) | (((N) >> 4) << 14))

__device__ __forceinline__ u32x4 ld_nt(__amdgpu_buffer_rsrc_t r, int off, int soff = 0) { return __builtin_amdgcn_raw_buffer_load_b128(r, off, soff, AUX_NT); }
__device__ __forceinline__ u32x4 ld_sc1(__amdgpu_buffer_rsrc_t r, int off) {
  asm volatile("" ::: "memory");
  return __builtin_amdgcn_raw_buffer_load_b128(r, off, 0, AUX_SC1);
}
__device__ __forceinline__ void st_sc1(__amdgpu_buffer_rsrc_t r, int off, u32x4 v) { __builtin_amdgcn_raw_buffer_store_b128(v, r, off, 0, AUX_SC1); }
__device__ __forceinline__ float asf(u32 x) { return __builtin_bit_cast(float, x); }
__device__ __forceinline__ u32 asu(float x) { return __builtin_bit_cast(u32, x); }
__device__ __forceinline__ u32 cvtpk(float lo, float hi) {
  f32x2 v = {lo, hi};
  return __builtin_bit_cast(u32, __builtin_convertvector(v, bf16x2));
}
__device__ __forceinline__ float dot2(u32 a, u32 b, float c) {
  return __builtin_amdgcn_fdot2_f32_bf16(__builtin_bit_cast(bf16x2, a), __builtin_bit_cast(bf16x2, b), c, false);
}
template <int CTRL> __device__ __forceinline__ float dpp(float x) { return asf((u32)__builtin_amdgcn_mov_dpp((int)asu(x), CTRL, 0xf, 0xf, true)); }
template <int CTRL> __device__ __forceinline__ u32 dppu(u32 x) { return (u32)__builtin_amdgcn_mov_dpp((int)x, CTRL, 0xf, 0xf, true); }
constexpr int XOR1 = 0xB1, XOR2 = 0x4E, XOR7 = 0x141, XOR8 = 0x128, ROR4 = 0x124, BCAST = 0x150;
__device__ __forceinline__ float x16_sum(float x) { auto s = __builtin_amdgcn_permlane16_swap(asu(x), asu(x), false, false); return asf(s[0]) + asf(s[1]); }
__device__ __forceinline__ float x32_sum(float x) { auto s = __builtin_amdgcn_permlane32_swap(asu(x), asu(x), false, false); return asf(s[0]) + asf(s[1]); }
__device__ __forceinline__ float x16_max(float x) { auto s = __builtin_amdgcn_permlane16_swap(asu(x), asu(x), false, false); return fmaxf(asf(s[0]), asf(s[1])); }
__device__ __forceinline__ float x32_max(float x) { auto s = __builtin_amdgcn_permlane32_swap(asu(x), asu(x), false, false); return fmaxf(asf(s[0]), asf(s[1])); }
__device__ __forceinline__ f32x4 mfma(u32x4 a, u32x4 b, f32x4 c) {
  return __builtin_amdgcn_mfma_f32_16x16x32_bf16(__builtin_bit_cast(bf16x8, a), __builtin_bit_cast(bf16x8, b), c, 0, 0, 0);
}
struct Sel { u32 s[4]; };
__device__ __forceinline__ u32x4 afrag(const Sel& sl, u32 X) {
  u32x4 a;
  a.x = __builtin_amdgcn_perm(X, X, sl.s[0]); a.y = __builtin_amdgcn_perm(X, X, sl.s[1]);
  a.z = __builtin_amdgcn_perm(X, X, sl.s[2]); a.w = __builtin_amdgcn_perm(X, X, sl.s[3]);
  return a;
}
struct Tag { u32 lo8; u32 hi; };
__device__ __forceinline__ u32 gpack(float v, const Tag& t) { return __builtin_amdgcn_alignbyte(t.lo8, asu(v) + 0x80u, 1); }
__device__ __forceinline__ u32x4 gpair(float a, float b, const Tag& t) { u32x4 g = {gpack(a, t), t.hi, gpack(b, t), t.hi}; return g; }
__device__ __forceinline__ u32 gbad(u32x4 g, const Tag& t) {
  return (g.y ^ t.hi) | (g.w ^ t.hi) | ((g.x >> 24) ^ t.lo8) | ((g.z >> 24) ^ t.lo8);
}
__device__ __forceinline__ bool none(u32 bad) { return __builtin_amdgcn_ballot_w64(bad != 0u) == 0ull; }
__device__ __forceinline__ float gv(u32 x) { return asf(x << 8); }

constexpr int RSTR = 136;
constexpr int L_RED = 0;
constexpr int L_Q = 3 * NWAVE * RSTR;
constexpr int L_M = L_Q + 6 * 128;
constexpr int L_L = L_M + 8;
constexpr int L_CTL = L_L + 8;
constexpr int L_TOT = L_CTL + 4;

__device__ __forceinline__ f32x2 wg_reduce(const float* red, int w, int lane) {
  f32x2 v = *(const f32x2*)(red + (lane & 7) * RSTR + 16 * w + 2 * (lane >> 3));
  v.x += dpp<XOR1>(v.x); v.y += dpp<XOR1>(v.y);
  v.x += dpp<XOR2>(v.x); v.y += dpp<XOR2>(v.y);
  v.x += dpp<XOR7>(v.x); v.y += dpp<XOR7>(v.y);
  return v;
}

__device__ __forceinline__ int kv_off(int j, int w) { return (j >> 2) * 32768 + ((w + (j >> 2)) & 7) * 4096 + (j & 3) * 1024; }

struct Pre { u32x2 xa, xb, ep; u32x4 q0, q1, k0, k1; };

template <bool ISA>
__device__ __forceinline__ void body(const u16* kp, const u16* vp, const u16* wqp, const u16* wop, u32* out, void* ws, float* lds,
                                     const int role, const int h, const int w, const int lane, const Pre& pre) {
  constexpr int NT = ISA ? 2 : 1;
  constexpr int NO = 4 * NT;
  constexpr int NQ = 4 * NT;
  constexpr int NU = ISA ? 16 : 22;
  constexpr int NBLK = ISA ? 32 : 128;
  constexpr int br = ISA ? 0 : 1;
  const int g = lane >> 4, c = lane & 15;
  const int tb = ISA ? 0 : (role < 5 ? 1 : 0);
  const int tk = ISA ? (role - 6) * 2 : (role < 5 ? role : 4);
  const int split = ISA ? role - 6 : role;
  const int qslot = ISA ? split : (role < 5 ? role : 2);

  const auto rq = RSRC(wqp + (size_t)tk * (128 * 4096) + h * 128, NQ * 32 * 8192);
  const auto ro = RSRC(wop + (size_t)h * (128 * 640) + tk * 128, 128 * 1280);
  const size_t kvoff = (size_t)h * (4096 * 128) + (ISA ? 3072 * 128 : 0);
  const auto rk = RSRC(kp + kvoff, NBLK * 8192);
  const auto rv = RSRC(vp + kvoff, NBLK * 8192);
  const auto rw = RSRC(ws, WS_END);

  const u32 xd[4] = {pre.xa.x, pre.xa.y, pre.xb.x, pre.xb.y};
  u32x4 WQ[NQ], WO[NO], K[NU], V[NU];
  const int qoff = (w * 4 + g) * NQ * 8192 + c * 16;
  const int ooff = (w * 16 + g * 4) * 1280 + c * 16;
  const int koff = lane * 16;
  const int blk0 = split * (ISA ? 16 : 21) + h * STG;
  const int xe = w * 6 + split;
  const bool xlive = !ISA && xe < 16;
  const int xso = kv_off((126 + (xe >> 3) + h * STG) & 127, xe & 7), xvo = xlive ? koff : OOB;
  constexpr int I_K = NQ + 2, I_V = NQ + NU, I_END = I_V + NU;
  WQ[0] = pre.q0; WQ[1] = pre.q1; K[0] = pre.k0; K[1] = pre.k1;
  auto issue = [&](int i) {
    if (i < I_K) WQ[i - 2] = ld_nt(rq, qoff + (i - 2) * 8192);
    else if (!ISA && i == I_V - 1) K[NU - 1] = ld_nt(rk, xvo, xso);
    else if (!ISA && i == I_END - 1) V[NU - 1] = ld_nt(rv, xvo, xso);
    else if (i < I_V) K[i - NQ] = ld_nt(rk, koff, kv_off((blk0 + i - NQ) & (NBLK - 1), w));
    else if (i < I_END) V[i - I_V] = ld_nt(rv, koff, kv_off((blk0 + i - I_V) & (NBLK - 1), w));
    SB();
  };
  auto step = [&](int i) { if (i < I_END) { WAITVM(WIN - 1); issue(i); } };
#pragma unroll
  for (int i = 4; i < WIN; ++i) issue(i);
  u32 pt = 0;
  if (w < 3) {
    const char* a = w == 0 ? (const char*)(vp + kvoff) : w == 1 ? (const char*)(wop + (size_t)h * (128 * 640)) : (const char*)out;
    const u32 o = w == 1 ? (lane & 1) * (128 * 1280 - 64) : 0u;
    if (lane < 2) pt = __hip_atomic_load((const GAS u32*)(a + o), __ATOMIC_RELAXED, __HIP_MEMORY_SCOPE_AGENT);
  } else if (w < 5) {
    u64 pc; int len;
    asm volatile("s_getpc_b64 %0\n\ts_mov_b32 %1, fd_code_end-." : "=s"(pc), "=s"(len));
    const int o = ((w - 3) * 64 + lane) * 128;
    if (o < len) pt = *(const GAS u32*)(pc + (u32)o);
  }
  SB();
  Sel sel;
  {
    const u32 src = (c & 8) ? 0x0100u : 0x0302u;
    const u32 m = (c & 1) ? ((src << 16) | 0x0c0cu) : (0x0c0c0000u | src);
#pragma unroll
    for (int i = 0; i < 4; ++i) sel.s[i] = (((c & 7) >> 1) == i) ? m : 0x0c0c0c0cu;
  }
  volatile LAS u32* ctl = (volatile LAS u32*)lds + L_CTL;
  if (threadIdx.x < 4) ctl[threadIdx.x] = 0u;
  asm volatile("s_waitcnt lgkmcnt(0)\n\ts_barrier" ::: "memory");
  SB();

  f32x4 acc = {0.f, 0.f, 0.f, 0.f};
  constexpr int I_S = I_K + WIN;
#pragma unroll
  for (int i = 0; i < I_K; ++i) {
    WAITVM(WIN - 1);
    if (i < 2 || i >= 4) {
      const int t = i < 2 ? i : i - 2;
      const u32 X = (t & 1) ? xd[t >> 1] : (xd[t >> 1] << 16);
      acc = mfma(afrag(sel, X), WQ[t], acc);
    }
    SB();
    issue(i + WIN);
  }
  if (lane < 32) *(f32x4*)(lds + L_RED + w * RSTR + 8 * c + 4 * g) = acc;
  asm volatile("s_waitcnt lgkmcnt(0)" ::: "memory");
  if (lane == 0) __hip_atomic_fetch_add((LAS u32*)ctl + 1, 1u, __ATOMIC_RELAXED, __HIP_MEMORY_SCOPE_WORKGROUP);
  SB();
  const u64 ep = (((u64)__builtin_amdgcn_readfirstlane(pre.ep.y) << 32) | (u32)__builtin_amdgcn_readfirstlane(pre.ep.x)) + 1ull;
  Tag tag; tag.lo8 = (u32)ep & 0xffu; tag.hi = (u32)(ep >> 8);
  SB();

  constexpr int NQP = ISA ? 3 : 5;
  constexpr int I_P = I_S + 2;
  constexpr int I_G = I_V - (ISA ? 2 : 6);
  constexpr int I_GC = I_G + WIN;
  constexpr int I_QK = I_V + (ISA ? 7 : WIN);
  static_assert(I_P < I_G && I_GC < I_QK && I_QK >= I_V + WIN, "order");
  u32x4 G;
  const int goff = WS_Q + ((br * 32 + h) * 6 + w) * 1024 + lane * 16;
#pragma unroll
  for (int i = I_S; i < I_QK; ++i) {
    step(i);
    if (i == I_P) {
      while (ctl[1] != (u32)NWAVE) __builtin_amdgcn_s_sleep(1);
      asm volatile("" ::: "memory");
      const f32x2 v = wg_reduce(lds + L_RED, w, lane);
      if ((lane & 7) == 0) st_sc1(rw, WS_Q + ((tb * 32 + h) * 6 + qslot) * 1024 + (16 * w + 2 * (lane >> 3)) * 8, gpair(v.x, v.y, tag));
      asm volatile("" :: "v"(pt));
      SB();
    }
    if (i == I_G && w < NQP) { G = ld_sc1(rw, goff); SB(); }
    if (i == I_GC && w < NQP) {
      if (!none(gbad(G, tag))) {
        for (u32 spin = 0; spin < SPIN_CAP; ++spin) {
          G = ld_sc1(rw, goff);
          if (none(gbad(G, tag))) break;
          __builtin_amdgcn_s_sleep(4);
        }
      }
      *(f32x2*)(lds + L_Q + w * 128 + 2 * lane) = f32x2{gv(G.x), gv(G.z)};
      asm volatile("s_waitcnt lgkmcnt(0)" ::: "memory");
      if (lane == 0) __hip_atomic_fetch_add((LAS u32*)ctl, 1u, __ATOMIC_RELAXED, __HIP_MEMORY_SCOPE_WORKGROUP);
      SB();
    }
  }
  while (ctl[0] != (u32)NQP) __builtin_amdgcn_s_sleep(1);
  asm volatile("" ::: "memory");
  u32x4 q;
  {
    f32x4 qa = *(const f32x4*)(lds + L_Q + 8 * c), qb = *(const f32x4*)(lds + L_Q + 8 * c + 4);
#pragma unroll
    for (int j = 1; j < NQP; ++j) { qa += *(const f32x4*)(lds + L_Q + j * 128 + 8 * c); qb += *(const f32x4*)(lds + L_Q + j * 128 + 8 * c + 4); }
    q.x = cvtpk(qa.x, qa.y); q.y = cvtpk(qa.z, qa.w); q.z = cvtpk(qb.x, qb.y); q.w = cvtpk(qb.z, qb.w);
  }

  float x[NU];
  constexpr int QPS = 4;
  constexpr int NQS = (NU + QPS - 1) / QPS;
#pragma unroll
  for (int s = 0; s < NQS; ++s) {
    step(I_QK + s);
#pragma unroll
    for (int t = s * QPS; t < (s + 1) * QPS && t < NU; ++t)
      x[t] = dot2(K[t].w, q.w, dot2(K[t].z, q.z, dot2(K[t].y, q.y, dot2(K[t].x, q.x, 0.f))));
    SB();
  }
  constexpr int I_R = I_QK + NQS;
  step(I_R);
  const bool b3 = c & 8, b2 = c & 4, b1 = c & 2, b0 = c & 1;
  float s0, s1 = -INFINITY;
  {
    float y[8];
#pragma unroll
    for (int i = 0; i < 8; ++i) y[i] = (b3 ? x[i + 8] : x[i]) + dpp<XOR8>(b3 ? x[i] : x[i + 8]);
#pragma unroll
    for (int i = 0; i < 4; ++i) y[i] = (b2 ? y[i + 4] : y[i]) + dpp<XOR7>(b2 ? y[i] : y[i + 4]);
#pragma unroll
    for (int i = 0; i < 2; ++i) y[i] = (b1 ? y[i + 2] : y[i]) + dpp<XOR2>(b1 ? y[i] : y[i + 2]);
    s0 = (b0 ? y[1] : y[0]) + dpp<XOR1>(b0 ? y[0] : y[1]);
  }
  SB();
  step(I_R + 1);
  if constexpr (NU > 16) {
    float y[4];
#pragma unroll
    for (int i = 0; i < 4; ++i) {
      if (20 + i < NU) y[i] = (b3 ? x[20 + i] : x[16 + i]) + dpp<XOR8>(b3 ? x[16 + i] : x[20 + i]);
      else y[i] = x[16 + i] + dpp<XOR8>(x[16 + i]);
    }
#pragma unroll
    for (int i = 0; i < 2; ++i) y[i] = (b2 ? y[i + 2] : y[i]) + dpp<XOR7>(b2 ? y[i] : y[i + 2]);
    const float z = (b1 ? y[1] : y[0]) + dpp<XOR2>(b1 ? y[0] : y[1]);
    s1 = z + dpp<XOR1>(z);
  }
  {
    const u32 r = cvtpk(s0, s1);
    const u32 r2 = cvtpk(asf(r << 16) * 0.08838834764831845f, asf(r & 0xffff0000u) * 0.08838834764831845f);
    s0 = asf(r2 << 16); s1 = asf(r2 & 0xffff0000u);
    if ((c >> 1) >= (xlive ? 6 : ISA ? 0 : 5)) s1 = -INFINITY;
  }
  float m = fmaxf(s0, s1);
  m = fmaxf(m, dpp<XOR1>(m)); m = fmaxf(m, dpp<XOR2>(m)); m = fmaxf(m, dpp<XOR7>(m)); m = fmaxf(m, dpp<XOR8>(m));
  m = x32_max(x16_max(m));
  if (lane == 0) {
    lds[L_M + w] = m;
    asm volatile("s_waitcnt lgkmcnt(0)" ::: "memory");
    __hip_atomic_fetch_add((LAS u32*)ctl + 2, 1u, __ATOMIC_RELAXED, __HIP_MEMORY_SCOPE_WORKGROUP);
  }
  const float p0 = __builtin_amdgcn_exp2f((s0 - m) * LOG2E), p1 = __builtin_amdgcn_exp2f((s1 - m) * LOG2E);
  const u32 P0 = cvtpk(p0 - asf(cvtpk(p0, p0) & 0xffff0000u), p0);
  const u32 P1 = cvtpk(p1 - asf(cvtpk(p1, p1) & 0xffff0000u), p1);
  float l = p0 + (b0 ? 0.f : p1);
  l += dpp<XOR1>(l); l += dpp<XOR2>(l); l += dpp<XOR7>(l); l += dpp<XOR8>(l);
  l = x32_sum(x16_sum(l));
  SB();
  const bool live = (c >> 1) < (tb ? 6 : 2);
  const int aoff = WS_P + ((tb * 32 + h) * 8 + (c >> 1)) * 1024 + (16 * w + 4 * g + 2 * (c & 1)) * 8;
  const int moff = WS_ML + ((tb * 32 + h) * 8 + (c >> 1)) * 128;
  const int b = blockIdx.x;
  const bool red = (w == 0 || (w == 1 && b < 64));
  const int quad = w == 0 ? b : 256 + b;
  const int lo = (lane >> 1) * (640 * 8) + (lane & 1) * 16;
  const int yoff = WS_Y + (quad >= 160 ? 32 * 640 * 8 + (quad - 160) * 32 : quad * 32) + lo;
  u32x4 a = ld_sc1(rw, live ? aoff : OOB), ml = ld_sc1(rw, live ? moff : OOB), y = {0u, 0u, 0u, 0u};
  if (red) y = ld_sc1(rw, yoff);
  SB();

  acc = f32x4{0.f, 0.f, 0.f, 0.f};
  constexpr int I_PV = I_R + 2, NPS = I_END - I_PV;
  constexpr int NEARLY = NU - WIN;
  static_assert(NPS >= 0 && NPS <= NEARLY, "every remaining load gets its step");
  float M = 0.f, sc = 0.f;
#define PV(t) if constexpr ((t) < NU) { \
    if constexpr ((t) < NEARLY && ((t) * NPS) % NEARLY < NPS) { SB(); step(I_PV + ((t) * NPS) / NEARLY); } \
    if constexpr ((t) == NEARLY) { \
      SB(); \
      while (ctl[2] != (u32)NWAVE) __builtin_amdgcn_s_sleep(1); \
      asm volatile("" ::: "memory"); \
      const f32x4 ma = *(const f32x4*)(lds + L_M), mb = *(const f32x4*)(lds + L_M + 4); \
      M = fmaxf(fmaxf(fmaxf(ma.x, ma.y), fmaxf(ma.z, ma.w)), fmaxf(fmaxf(mb.x, mb.y), fmaxf(mb.z, mb.w))); \
      sc = __builtin_amdgcn_exp2f((m - M) * LOG2E); \
      if (lane == 0) lds[L_L + w] = l * sc; \
      SB(); \
    } \
    const u32 X = (t) < 16 ? dppu<BCAST + ((t) & 15)>(P0) : dppu<BCAST + ((2 * (t)) & 15)>(P1); \
    acc = mfma(afrag(sel, X), V[t], acc); \
    if constexpr ((t) == NU - 2) { \
      SB(); \
      _Pragma("unroll") for (int o = 0; o < NO; ++o) WO[o] = ld_nt(ro, ooff + (o & 3) * 1280 + (o >> 2) * 256); \
      SB(); \
    } }
  PV(0) PV(1) PV(2) PV(3) PV(4) PV(5) PV(6) PV(7) PV(8) PV(9) PV(10) PV(11) PV(12) PV(13) PV(14) PV(15) PV(16) PV(17) PV(18) PV(19) PV(20) PV(21)
#undef PV
  {
    auto u = __builtin_amdgcn_permlane32_swap(asu(acc.x), asu(acc.z), false, false);
    auto v = __builtin_amdgcn_permlane32_swap(asu(acc.y), asu(acc.w), false, false);
    const f32x2 r = {(asf(u[0]) + asf(u[1])) * sc, (asf(v[0]) + asf(v[1])) * sc};
    *(f32x2*)(lds + L_RED + w * RSTR + 8 * c + 4 * (g & 1) + 2 * (g >> 1)) = r;
  }
  asm volatile("s_waitcnt lgkmcnt(0)\n\ts_barrier" ::: "memory");
  {
    const f32x2 v = wg_reduce(lds + L_RED, w, lane);
    float L = lds[L_L + (lane & 7)];
    L += dpp<XOR1>(L); L += dpp<XOR2>(L); L += dpp<XOR7>(L);
    const int slot = (br * 32 + h) * 8 + split;
    if ((lane & 7) == 0) st_sc1(rw, WS_P + slot * 1024 + (16 * w + 2 * (lane >> 3)) * 8, gpair(v.x, v.y, tag));
    if (threadIdx.x < 8) st_sc1(rw, WS_ML + slot * 128 + lane * 16, gpair(M, L, tag));
  }

  u32 Y;
  {
    bool na = live && gbad(a, tag) != 0u, nm = live && gbad(ml, tag) != 0u;
    for (u32 spin = 0; spin < SPIN_CAP; ++spin) {
      if (na) a = ld_sc1(rw, aoff);
      if (nm) ml = ld_sc1(rw, moff);
      na = live && gbad(a, tag) != 0u; nm = live && gbad(ml, tag) != 0u;
      if (__builtin_amdgcn_ballot_w64(na || nm) == 0ull) break;
    }
    const float ms = live ? gv(ml.x) : -INFINITY;
    float Mx = ms;
    Mx = fmaxf(Mx, dpp<XOR2>(Mx)); Mx = fmaxf(Mx, dpp<XOR8>(Mx)); Mx = fmaxf(Mx, dpp<ROR4>(Mx));
    const float wt = __builtin_amdgcn_exp2f((ms - Mx) * LOG2E);
    float L = wt * gv(ml.z), n0 = wt * gv(a.x), n1 = wt * gv(a.z);
    L += dpp<XOR2>(L); n0 += dpp<XOR2>(n0); n1 += dpp<XOR2>(n1);
    L += dpp<XOR8>(L); n0 += dpp<XOR8>(n0); n1 += dpp<XOR8>(n1);
    L += dpp<ROR4>(L); n0 += dpp<ROR4>(n0); n1 += dpp<ROR4>(n1);
    const float inv = __builtin_amdgcn_rcpf(L);
    Y = cvtpk(n0 * inv, n1 * inv);
  }
  {
    const u32 Y0 = dppu<0xA0>(Y), Y1 = dppu<0xF5>(Y);
    const u32x4 a0 = afrag(sel, Y0 << 16), a1 = afrag(sel, Y0), a2 = afrag(sel, Y1 << 16), a3 = afrag(sel, Y1);
#pragma unroll
    for (int k = 0; k < NT; ++k) {
      f32x4 d = {0.f, 0.f, 0.f, 0.f};
      d = mfma(a0, WO[4 * k + 0], d); d = mfma(a1, WO[4 * k + 1], d); d = mfma(a2, WO[4 * k + 2], d); d = mfma(a3, WO[4 * k + 3], d);
      if (lane < 32) *(f32x4*)(lds + L_RED + ((k + 1) * NWAVE + w) * RSTR + 8 * c + 4 * g) = d;
    }
  }
  asm volatile("s_waitcnt lgkmcnt(0)\n\ts_barrier" ::: "memory");
#pragma unroll
  for (int k = 0; k < NT; ++k) {
    const f32x2 v = wg_reduce(lds + L_RED + (k + 1) * NWAVE * RSTR, w, lane);
    if ((lane & 7) == 0) st_sc1(rw, WS_Y + ((tb * 32 + h) * 640 + (tk + k) * 128 + 16 * w + 2 * (lane >> 3)) * 8, gpair(v.x, v.y, tag));
  }

  if (red) {
    const bool closer = (w == 1 && b == 0);
    u32x4 z = {0u, 0u, 0u, 0u};
    bool ny = gbad(y, tag) != 0u, nz = closer;
    for (u32 spin = 0; spin < SPIN_CAP; ++spin) {
      if (ny) y = ld_sc1(rw, yoff);
      if (nz) z = ld_sc1(rw, WS_Y + lo);
      ny = gbad(y, tag) != 0u; nz = closer && gbad(z, tag) != 0u;
      if (__builtin_amdgcn_ballot_w64(ny || nz) == 0ull) break;
    }
    float o0 = gv(y.x), o1 = gv(y.z);
    o0 += dpp<XOR2>(o0); o1 += dpp<XOR2>(o1);
    o0 += dpp<XOR8>(o0); o1 += dpp<XOR8>(o1);
    o0 += dpp<ROR4>(o0); o1 += dpp<ROR4>(o1);
    o0 = x32_sum(x16_sum(o0)); o1 = x32_sum(x16_sum(o1));
    if (lane < 2) __hip_atomic_store((GAS u32*)out + quad * 2 + lane, cvtpk(o0, o1), __ATOMIC_RELAXED, __HIP_MEMORY_SCOPE_AGENT);
    if (closer && lane == 0) __hip_atomic_store((GAS u64*)((char*)ws + WS_EPOCH), ep, __ATOMIC_RELAXED, __HIP_MEMORY_SCOPE_AGENT);
  }
}

__global__ void __launch_bounds__(NTHR, 2) fused_decode(Params p) {
  __shared__ __attribute__((aligned(16))) float lds[L_TOT];
  const int b = blockIdx.x, role = b >> 5, h = b & 31;
  const int lane = threadIdx.x & 63, w = __builtin_amdgcn_readfirstlane(threadIdx.x >> 6);
  const bool isA = role >= 6;
  const int nq = isA ? 8 : 4;
  const int qrow0 = ((0x20443210u >> (4 * role)) & 15) * 128;
  const int blk0 = (int)((0x100069543F2A1500ull >> (8 * role)) & 255) + h * STG, msk = isA ? 31 : 127;
  const int r0 = qrow0 + (w * 4 + (lane >> 4)) * nq;
  const u32 kvo = (u32)h * (4096 * 256) + (isA ? 3072 * 256 : 0);
  int xo = r0 * 2, xo2 = r0 * 2 + 8, qo = r0 * 8192 + (lane & 15) * 16, ko = lane * 16;
  int so0 = kvo + kv_off(blk0 & msk, w), so1 = kvo + kv_off((blk0 + 1) & msk, w), sq0 = h * 256, sq1 = h * 256 + 8192, role2 = role;
  asm volatile("" : "+v"(xo), "+v"(xo2), "+v"(qo), "+v"(ko), "+s"(so0), "+s"(so1), "+s"(sq0), "+s"(sq1), "+s"(role2));
  SB();
  const bool isA2 = role2 >= 6, tB2 = role2 < 5;
  const u16* kp = isA2 ? p.kA : p.kB;
  const u16* vp = isA2 ? p.vA : p.vB;
  const u16* wqp = tB2 ? p.wqB : p.wqA;
  const u16* xp = tB2 ? p.xB : p.xA;
  const u16* wop = tB2 ? p.woB : p.woA;
  u32* out = p.out;
  void* ws = p.ws;
  asm volatile("" : "+s"(kp), "+s"(vp), "+s"(wqp), "+s"(wop), "+s"(xp), "+s"(out), "+s"(ws));
  Pre pre;
  {
    const auto rq = RSRC(wqp, 640 * 8192);
    const auto rx = RSRC(xp, 1280);
    const auto rk = RSRC(kp, 32 * 4096 * 256);
    pre.xa = __builtin_amdgcn_raw_buffer_load_b64(rx, xo, 0, 0);
    pre.xb = __builtin_amdgcn_raw_buffer_load_b64(rx, xo2, 0, 0);
    pre.ep = __builtin_amdgcn_raw_buffer_load_b64(RSRC(ws, 64), WS_EPOCH, 0, AUX_SC1);
    SB();
    pre.q0 = ld_nt(rq, qo, sq0);
    pre.q1 = ld_nt(rq, qo, sq1);
    pre.k0 = ld_nt(rk, ko, so0);
    pre.k1 = ld_nt(rk, ko, so1);
    SB();
  }
  if (isA) body<true>(kp, vp, wqp, wop, out, ws, lds, role, h, w, lane, pre);
  else body<false>(kp, vp, wqp, wop, out, ws, lds, role, h, w, lane, pre);
  asm volatile("fd_code_end:");
}

extern "C" void kernel_launch(void* const* d_in, const int* in_sizes, int n_in, void* d_out, int out_size, void* d_ws, size_t ws_size,
                              hipStream_t stream) {
  Params p;
  p.xA = (const u16*)d_in[0]; p.kA = (const u16*)d_in[1]; p.vA = (const u16*)d_in[2]; p.wqA = (const u16*)d_in[3]; p.woA = (const u16*)d_in[4];
  p.xB = (const u16*)d_in[5]; p.kB = (const u16*)d_in[6]; p.vB = (const u16*)d_in[7]; p.wqB = (const u16*)d_in[8]; p.woB = (const u16*)d_in[9];
  p.out = (u32*)d_out; p.ws = d_ws;
  fused_decode<<<256, NTHR, 0, stream>>>(p);
}
```

```cpp
#include <hip/hip_runtime.h>
#include <hip/hip_bf16.h>
#include <stdint.h>

typedef unsigned int u32;
typedef unsigned short u16;
typedef unsigned long long u64;
typedef u32 u32x2 __attribute__((ext_vector_type(2)));
typedef u32 u32x4 __attribute__((ext_vector_type(4)));
typedef float f32x2 __attribute__((ext_vector_type(2)));
typedef float f32x4 __attribute__((ext_vector_type(4)));
typedef __bf16 bf16x2 __attribute__((ext_vector_type(2)));
typedef __bf16 bf16x8 __attribute__((ext_vector_type(8)));
#define LAS __attribute__((address_space(3)))
#define GAS __attribute__((address_space(1)))

constexpr int NWAVE = 8, NTHR = NWAVE * 64;
constexpr int WIN = 5;
constexpr int STG = 4;
constexpr int OOB = 0x7fffff00;
constexpr int AUX_NT = 2, AUX_SC1 = 16;
constexpr u32 SPIN_CAP = 200000;
constexpr float LOG2E = 1.4426950408889634f;

constexpr int WS_EPOCH = 0;
constexpr int WS_Q = 4096;
constexpr int WS_P = WS_Q + 64 * 6 * 1024;
constexpr int WS_ML = WS_P + 64 * 8 * 1024;
constexpr int WS_Y = WS_ML + 64 * 8 * 128;
constexpr int WS_END = WS_Y + 64 * 640 * 8;

struct Params {
  const u16 *xA, *kA, *vA, *wqA, *woA, *xB, *kB, *vB, *wqB, *woB;
  u32* out;
  void* ws;
};

#define RSRC(ptr, n) __builtin_amdgcn_make_buffer_rsrc((void*)(ptr), 0, (n), 0x00020000)
#define SB() __builtin_amdgcn_sched_barrier(0)
#define WAITVM(N) __builtin_amdgcn_s_waitcnt(((N) & 15) | (7 << 4) | (15 << 8) | (((N) >> 4) << 14))

__device__ __forceinline__ u32x4 ld_nt(__amdgpu_buffer_rsrc_t r, int off, int soff = 0) { return __builtin_amdgcn_raw_buffer_load_b128(r, off, soff, AUX_NT); }
__device__ __forceinline__ u32x4 ld_sc1(__amdgpu_buffer_rsrc_t r, int off) {
  asm volatile("" ::: "memory");
  return __builtin_amdgcn_raw_buffer_load_b128(r, off, 0, AUX_SC1);
}
__device__ __forceinline__ void st_sc1(__amdgpu_buffer_rsrc_t r, int off, u32x4 v) { __builtin_amdgcn_raw_buffer_store_b128(v, r, off, 0, AUX_SC1); }
__device__ __forceinline__ float asf(u32 x) { return __builtin_bit_cast(float, x); }
__device__ __forceinline__ u32 asu(float x) { return __builtin_bit_cast(u32, x); }
__device__ __forceinline__ u32 cvtpk(float lo, float hi) {
  f32x2 v = {lo, hi};
  return __builtin_bit_cast(u32, __builtin_convertvector(v, bf16x2));
}
__device__ __forceinline__ float dot2(u32 a, u32 b, float c) {
  return __builtin_amdgcn_fdot2_f32_bf16(__builtin_bit_cast(bf16x2, a), __builtin_bit_cast(bf16x2, b), c, false);
}
template <int CTRL> __device__ __forceinline__ float dpp(float x) { return asf((u32)__builtin_amdgcn_mov_dpp((int)asu(x), CTRL, 0xf, 0xf, true)); }
template <int CTRL> __device__ __forceinline__ u32 dppu(u32 x) { return (u32)__builtin_amdgcn_mov_dpp((int)x, CTRL, 0xf, 0xf, true); }
constexpr int XOR1 = 0xB1, XOR2 = 0x4E, XOR7 = 0x141, XOR8 = 0x128, ROR4 = 0x124, BCAST = 0x150;
__device__ __forceinline__ float x16_sum(float x) { auto s = __builtin_amdgcn_permlane16_swap(asu(x), asu(x), false, false); return asf(s[0]) + asf(s[1]); }
__device__ __forceinline__ float x32_sum(float x) { auto s = __builtin_amdgcn_permlane32_swap(asu(x), asu(x), false, false); return asf(s[0]) + asf(s[1]); }
__device__ __forceinline__ float x16_max(float x) { auto s = __builtin_amdgcn_permlane16_swap(asu(x), asu(x), false, false); return fmaxf(asf(s[0]), asf(s[1])); }
__device__ __forceinline__ float x32_max(float x) { auto s = __builtin_amdgcn_permlane32_swap(asu(x), asu(x), false, false); return fmaxf(asf(s[0]), asf(s[1])); }
__device__ __forceinline__ f32x4 mfma(u32x4 a, u32x4 b, f32x4 c) {
  return __builtin_amdgcn_mfma_f32_16x16x32_bf16(__builtin_bit_cast(bf16x8, a), __builtin_bit_cast(bf16x8, b), c, 0, 0, 0);
}
struct Sel { u32 s[4]; };
__device__ __forceinline__ u32x4 afrag(const Sel& sl, u32 X) {
  u32x4 a;
  a.x = __builtin_amdgcn_perm(X, X, sl.s[0]); a.y = __builtin_amdgcn_perm(X, X, sl.s[1]);
  a.z = __builtin_amdgcn_perm(X, X, sl.s[2]); a.w = __builtin_amdgcn_perm(X, X, sl.s[3]);
  return a;
}
struct Tag { u32 lo8; u32 hi; };
__device__ __forceinline__ u32 gpack(float v, const Tag& t) { return __builtin_amdgcn_alignbyte(t.lo8, asu(v) + 0x80u, 1); }
__device__ __forceinline__ u32x4 gpair(float a, float b, const Tag& t) { u32x4 g = {gpack(a, t), t.hi, gpack(b, t), t.hi}; return g; }
__device__ __forceinline__ u32 gbad(u32x4 g, const Tag& t) {
  return (g.y ^ t.hi) | (g.w ^ t.hi) | ((g.x >> 24) ^ t.lo8) | ((g.z >> 24) ^ t.lo8);
}
__device__ __forceinline__ bool none(u32 bad) { return __builtin_amdgcn_ballot_w64(bad != 0u) == 0ull; }
__device__ __forceinline__ float gv(u32 x) { return asf(x << 8); }

constexpr int RSTR = 136;
constexpr int L_RED = 0;
constexpr int L_Q = 3 * NWAVE * RSTR;
constexpr int L_M = L_Q + 6 * 128;
constexpr int L_L = L_M + 8;
constexpr int L_CTL = L_L + 8;
constexpr int L_TOT = L_CTL + 4;

__device__ __forceinline__ f32x2 wg_reduce(const float* red, int w, int lane) {
  f32x2 v = *(const f32x2*)(red + (lane & 7) * RSTR + 16 * w + 2 * (lane >> 3));
  v.x += dpp<XOR1>(v.x); v.y += dpp<XOR1>(v.y);
  v.x += dpp<XOR2>(v.x); v.y += dpp<XOR2>(v.y);
  v.x += dpp<XOR7>(v.x); v.y += dpp<XOR7>(v.y);
  return v;
}

__device__ __forceinline__ int kv_off(int j, int w) { return (j >> 2) * 32768 + ((w + (j >> 2)) & 7) * 4096 + (j & 3) * 1024; }

struct Pre { u32x2 xa, xb, ep; u32x4 q0, q1, k0, k1; };

template <bool ISA>
__device__ __forceinline__ void body(const u16* kp, const u16* vp, const u16* wqp, const u16* wop, u32* out, void* ws, float* lds,
                                     const int role, const int h, const int w, const int lane, const Pre& pre, const u64 tpad0) {
  constexpr int NT = ISA ? 2 : 1;
  constexpr int NO = 4 * NT;
  constexpr int NQ = 4 * NT;
  constexpr int NU = ISA ? 16 : 22;
  constexpr int NBLK = ISA ? 32 : 128;
  constexpr int br = ISA ? 0 : 1;
  const int g = lane >> 4, c = lane & 15;
  const int tb = ISA ? 0 : (role < 5 ? 1 : 0);
  const int tk = ISA ? (role - 6) * 2 : (role < 5 ? role : 4);
  const int split = ISA ? role - 6 : role;
  const int qslot = ISA ? split : (role < 5 ? role : 2);

  const auto rq = RSRC(wqp + (size_t)tk * (128 * 4096) + h * 128, NQ * 32 * 8192);
  const auto ro = RSRC(wop + (size_t)h * (128 * 640) + tk * 128, 128 * 1280);
  const size_t kvoff = (size_t)h * (4096 * 128) + (ISA ? 3072 * 128 : 0);
  const auto rk = RSRC(kp + kvoff, NBLK * 8192);
  const auto rv = RSRC(vp + kvoff, NBLK * 8192);
  const auto rw = RSRC(ws, WS_END);

  const u32 xd[4] = {pre.xa.x, pre.xa.y, pre.xb.x, pre.xb.y};
  u32x4 WQ[NQ], WO[NO], K[NU], V[NU];
  const int qoff = (w * 4 + g) * NQ * 8192 + c * 16;
  const int ooff = (w * 16 + g * 4) * 1280 + c * 16;
  const int koff = lane * 16;
  const int blk0 = split * (ISA ? 16 : 21) + h * STG;
  const int xe = w * 6 + split;
  const bool xlive = !ISA && xe < 16;
  const int xso = kv_off((126 + (xe >> 3) + h * STG) & 127, xe & 7), xvo = xlive ? koff : OOB;
  constexpr int I_K = NQ + 2, I_V = NQ + NU, I_END = I_V + NU;
  WQ[0] = pre.q0; WQ[1] = pre.q1; K[0] = pre.k0; K[1] = pre.k1;
  auto issue = [&](int i) {
    if (i < I_K) WQ[i - 2] = ld_nt(rq, qoff + (i - 2) * 8192);
    else if (!ISA && i == I_V - 1) K[NU - 1] = ld_nt(rk, xvo, xso);
    else if (!ISA && i == I_END - 1) V[NU - 1] = ld_nt(rv, xvo, xso);
    else if (i < I_V) K[i - NQ] = ld_nt(rk, koff, kv_off((blk0 + i - NQ) & (NBLK - 1), w));
    else if (i < I_END) V[i - I_V] = ld_nt(rv, koff, kv_off((blk0 + i - I_V) & (NBLK - 1), w));
    SB();
  };
  auto step = [&](int i) { if (i < I_END) { WAITVM(WIN - 1); issue(i); } };
#pragma unroll
  for (int i = 4; i < WIN; ++i) issue(i);
  u32 pt = 0;
  if (w < 3) {
    const char* a = w == 0 ? (const char*)(vp + kvoff) : w == 1 ? (const char*)(wop + (size_t)h * (128 * 640)) : (const char*)out;
    const u32 o = w == 1 ? (lane & 1) * (128 * 1280 - 64) : 0u;
    if (lane < 2) pt = __hip_atomic_load((const GAS u32*)(a + o), __ATOMIC_RELAXED, __HIP_MEMORY_SCOPE_AGENT);
  }
  SB();
  Sel sel;
  {
    const u32 src = (c & 8) ? 0x0100u : 0x0302u;
    const u32 m = (c & 1) ? ((src << 16) | 0x0c0cu) : (0x0c0c0000u | src);
#pragma unroll
    for (int i = 0; i < 4; ++i) sel.s[i] = (((c & 7) >> 1) == i) ? m : 0x0c0c0c0cu;
  }
  volatile LAS u32* ctl = (volatile LAS u32*)lds + L_CTL;
  if (threadIdx.x < 4) ctl[threadIdx.x] = 0u;
  asm volatile("s_waitcnt lgkmcnt(0)\n\ts_barrier" ::: "memory");
  SB();

  f32x4 acc = {0.f, 0.f, 0.f, 0.f};
  constexpr int I_S = I_K + WIN;
#pragma unroll
  for (int i = 0; i < I_K; ++i) {
    WAITVM(WIN - 1);
    if (i < 2 || i >= 4) {
      const int t = i < 2 ? i : i - 2;
      const u32 X = (t & 1) ? xd[t >> 1] : (xd[t >> 1] << 16);
      acc = mfma(afrag(sel, X), WQ[t], acc);
    }
    SB();
    issue(i + WIN);
  }
  if (lane < 32) *(f32x4*)(lds + L_RED + w * RSTR + 8 * c + 4 * g) = acc;
  asm volatile("s_waitcnt lgkmcnt(0)" ::: "memory");
  if (lane == 0) __hip_atomic_fetch_add((LAS u32*)ctl + 1, 1u, __ATOMIC_RELAXED, __HIP_MEMORY_SCOPE_WORKGROUP);
  SB();
  const u64 ep = (((u64)__builtin_amdgcn_readfirstlane(pre.ep.y) << 32) | (u32)__builtin_amdgcn_readfirstlane(pre.ep.x)) + 1ull;
  Tag tag; tag.lo8 = (u32)ep & 0xffu; tag.hi = (u32)(ep >> 8);
  SB();

  constexpr int NQP = ISA ? 3 : 5;
  constexpr int I_P = I_S + 2;
  constexpr int I_G = I_V - (ISA ? 2 : 6);
  constexpr int I_GC = I_G + WIN;
  constexpr int I_QK = I_V + (ISA ? 7 : WIN);
  static_assert(I_P < I_G && I_GC < I_QK && I_QK >= I_V + WIN, "order");
  u32x4 G;
  const int goff = WS_Q + ((br * 32 + h) * 6 + w) * 1024 + lane * 16;
#pragma unroll
  for (int i = I_S; i < I_QK; ++i) {
    step(i);
    if (i == I_P) {
      while (ctl[1] != (u32)NWAVE) __builtin_amdgcn_s_sleep(1);
      asm volatile("" ::: "memory");
      const f32x2 v = wg_reduce(lds + L_RED, w, lane);
      if ((lane & 7) == 0) st_sc1(rw, WS_Q + ((tb * 32 + h) * 6 + qslot) * 1024 + (16 * w + 2 * (lane >> 3)) * 8, gpair(v.x, v.y, tag));
      asm volatile("" :: "v"(pt));
      SB();
    }
    if (i == I_G && w < NQP) { G = ld_sc1(rw, goff); SB(); }
    if (i == I_GC && w < NQP) {
      if (!none(gbad(G, tag))) {
        for (u32 spin = 0; spin < SPIN_CAP; ++spin) {
          G = ld_sc1(rw, goff);
          if (none(gbad(G, tag))) break;
          __builtin_amdgcn_s_sleep(4);
        }
      }
      *(f32x2*)(lds + L_Q + w * 128 + 2 * lane) = f32x2{gv(G.x), gv(G.z)};
      asm volatile("s_waitcnt lgkmcnt(0)" ::: "memory");
      if (lane == 0) __hip_atomic_fetch_add((LAS u32*)ctl, 1u, __ATOMIC_RELAXED, __HIP_MEMORY_SCOPE_WORKGROUP);
      SB();
    }
  }
  while (ctl[0] != (u32)NQP) __builtin_amdgcn_s_sleep(1);
  asm volatile("" ::: "memory");
  u32x4 q;
  {
    f32x4 qa = *(const f32x4*)(lds + L_Q + 8 * c), qb = *(const f32x4*)(lds + L_Q + 8 * c + 4);
#pragma unroll
    for (int j = 1; j < NQP; ++j) { qa += *(const f32x4*)(lds + L_Q + j * 128 + 8 * c); qb += *(const f32x4*)(lds + L_Q + j * 128 + 8 * c + 4); }
    q.x = cvtpk(qa.x, qa.y); q.y = cvtpk(qa.z, qa.w); q.z = cvtpk(qb.x, qb.y); q.w = cvtpk(qb.z, qb.w);
  }

  float x[NU];
  constexpr int QPS = 4;
  constexpr int NQS = (NU + QPS - 1) / QPS;
#pragma unroll
  for (int s = 0; s < NQS; ++s) {
    step(I_QK + s);
#pragma unroll
    for (int t = s * QPS; t < (s + 1) * QPS && t < NU; ++t)
      x[t] = dot2(K[t].w, q.w, dot2(K[t].z, q.z, dot2(K[t].y, q.y, dot2(K[t].x, q.x, 0.f))));
    SB();
  }
  constexpr int I_R = I_QK + NQS;
  step(I_R);
  const bool b3 = c & 8, b2 = c & 4, b1 = c & 2, b0 = c & 1;
  float s0, s1 = -INFINITY;
  {
    float y[8];
#pragma unroll
    for (int i = 0; i < 8; ++i) y[i] = (b3 ? x[i + 8] : x[i]) + dpp<XOR8>(b3 ? x[i] : x[i + 8]);
#pragma unroll
    for (int i = 0; i < 4; ++i) y[i] = (b2 ? y[i + 4] : y[i]) + dpp<XOR7>(b2 ? y[i] : y[i + 4]);
#pragma unroll
    for (int i = 0; i < 2; ++i) y[i] = (b1 ? y[i + 2] : y[i]) + dpp<XOR2>(b1 ? y[i] : y[i + 2]);
    s0 = (b0 ? y[1] : y[0]) + dpp<XOR1>(b0 ? y[0] : y[1]);
  }
  SB();
  step(I_R + 1);
  if constexpr (NU > 16) {
    float y[4];
#pragma unroll
    for (int i = 0; i < 4; ++i) {
      if (20 + i < NU) y[i] = (b3 ? x[20 + i] : x[16 + i]) + dpp<XOR8>(b3 ? x[16 + i] : x[20 + i]);
      else y[i] = x[16 + i] + dpp<XOR8>(x[16 + i]);
    }
#pragma unroll
    for (int i = 0; i < 2; ++i) y[i] = (b2 ? y[i + 2] : y[i]) + dpp<XOR7>(b2 ? y[i] : y[i + 2]);
    const float z = (b1 ? y[1] : y[0]) + dpp<XOR2>(b1 ? y[0] : y[1]);
    s1 = z + dpp<XOR1>(z);
  }
  {
    const u32 r = cvtpk(s0, s1);
    const u32 r2 = cvtpk(asf(r << 16) * 0.08838834764831845f, asf(r & 0xffff0000u) * 0.08838834764831845f);
    s0 = asf(r2 << 16); s1 = asf(r2 & 0xffff0000u);
    if ((c >> 1) >= (xlive ? 6 : ISA ? 0 : 5)) s1 = -INFINITY;
  }
  float m = fmaxf(s0, s1);
  m = fmaxf(m, dpp<XOR1>(m)); m = fmaxf(m, dpp<XOR2>(m)); m = fmaxf(m, dpp<XOR7>(m)); m = fmaxf(m, dpp<XOR8>(m));
  m = x32_max(x16_max(m));
  if (lane == 0) {
    lds[L_M + w] = m;
    asm volatile("s_waitcnt lgkmcnt(0)" ::: "memory");
    __hip_atomic_fetch_add((LAS u32*)ctl + 2, 1u, __ATOMIC_RELAXED, __HIP_MEMORY_SCOPE_WORKGROUP);
  }
  const float p0 = __builtin_amdgcn_exp2f((s0 - m) * LOG2E), p1 = __builtin_amdgcn_exp2f((s1 - m) * LOG2E);
  const u32 P0 = cvtpk(p0 - asf(cvtpk(p0, p0) & 0xffff0000u), p0);
  const u32 P1 = cvtpk(p1 - asf(cvtpk(p1, p1) & 0xffff0000u), p1);
  float l = p0 + (b0 ? 0.f : p1);
  l += dpp<XOR1>(l); l += dpp<XOR2>(l); l += dpp<XOR7>(l); l += dpp<XOR8>(l);
  l = x32_sum(x16_sum(l));
  SB();
  const bool live = (c >> 1) < (tb ? 6 : 2);
  const int aoff = WS_P + ((tb * 32 + h) * 8 + (c >> 1)) * 1024 + (16 * w + 4 * g + 2 * (c & 1)) * 8;
  const int moff = WS_ML + ((tb * 32 + h) * 8 + (c >> 1)) * 128;
  const int b = blockIdx.x;
  const bool red = (w == 0 || (w == 1 && b < 64));
  const int quad = w == 0 ? b : 256 + b;
  const int lo = (lane >> 1) * (640 * 8) + (lane & 1) * 16;
  const int yoff = WS_Y + (quad >= 160 ? 32 * 640 * 8 + (quad - 160) * 32 : quad * 32) + lo;
  u32x4 a = ld_sc1(rw, live ? aoff : OOB), ml = ld_sc1(rw, live ? moff : OOB), y = {0u, 0u, 0u, 0u};
  if (red) y = ld_sc1(rw, yoff);
  SB();

  acc = f32x4{0.f, 0.f, 0.f, 0.f};
  constexpr int I_PV = I_R + 2, NPS = I_END - I_PV;
  constexpr int NEARLY = NU - WIN;
  static_assert(NPS >= 0 && NPS <= NEARLY, "every remaining load gets its step");
  float M = 0.f, sc = 0.f;
#define PV(t) if constexpr ((t) < NU) { \
    if constexpr ((t) < NEARLY && ((t) * NPS) % NEARLY < NPS) { SB(); step(I_PV + ((t) * NPS) / NEARLY); } \
    if constexpr ((t) == NEARLY) { \
      SB(); \
      while (ctl[2] != (u32)NWAVE) __builtin_amdgcn_s_sleep(1); \
      asm volatile("" ::: "memory"); \
      const f32x4 ma = *(const f32x4*)(lds + L_M), mb = *(const f32x4*)(lds + L_M + 4); \
      M = fmaxf(fmaxf(fmaxf(ma.x, ma.y), fmaxf(ma.z, ma.w)), fmaxf(fmaxf(mb.x, mb.y), fmaxf(mb.z, mb.w))); \
      sc = __builtin_amdgcn_exp2f((m - M) * LOG2E); \
      if (lane == 0) lds[L_L + w] = l * sc; \
      SB(); \
    } \
    const u32 X = (t) < 16 ? dppu<BCAST + ((t) & 15)>(P0) : dppu<BCAST + ((2 * (t)) & 15)>(P1); \
    acc = mfma(afrag(sel, X), V[t], acc); \
    if constexpr ((t) == NU - 2) { \
      SB(); \
      _Pragma("unroll") for (int o = 0; o < NO; ++o) WO[o] = ld_nt(ro, ooff + (o & 3) * 1280 + (o >> 2) * 256); \
      SB(); \
    } }
  PV(0) PV(1) PV(2) PV(3) PV(4) PV(5) PV(6) PV(7) PV(8) PV(9) PV(10) PV(11) PV(12) PV(13) PV(14) PV(15) PV(16) PV(17) PV(18) PV(19) PV(20) PV(21)
#undef PV
  {
    auto u = __builtin_amdgcn_permlane32_swap(asu(acc.x), asu(acc.z), false, false);
    auto v = __builtin_amdgcn_permlane32_swap(asu(acc.y), asu(acc.w), false, false);
    const f32x2 r = {(asf(u[0]) + asf(u[1])) * sc, (asf(v[0]) + asf(v[1])) * sc};
    *(f32x2*)(lds + L_RED + w * RSTR + 8 * c + 4 * (g & 1) + 2 * (g >> 1)) = r;
  }
  asm volatile("s_waitcnt lgkmcnt(0)\n\ts_barrier" ::: "memory");
  {
    const f32x2 v = wg_reduce(lds + L_RED, w, lane);
    float L = lds[L_L + (lane & 7)];
    L += dpp<XOR1>(L); L += dpp<XOR2>(L); L += dpp<XOR7>(L);
    const int slot = (br * 32 + h) * 8 + split;
    if ((lane & 7) == 0) st_sc1(rw, WS_P + slot * 1024 + (16 * w + 2 * (lane >> 3)) * 8, gpair(v.x, v.y, tag));
    if (threadIdx.x < 8) st_sc1(rw, WS_ML + slot * 128 + lane * 16, gpair(M, L, tag));
  }

  u32 Y;
  {
    bool na = live && gbad(a, tag) != 0u, nm = live && gbad(ml, tag) != 0u;
    for (u32 spin = 0; spin < SPIN_CAP; ++spin) {
      if (na) a = ld_sc1(rw, aoff);
      if (nm) ml = ld_sc1(rw, moff);
      na = live && gbad(a, tag) != 0u; nm = live && gbad(ml, tag) != 0u;
      if (__builtin_amdgcn_ballot_w64(na || nm) == 0ull) break;
    }
    const float ms = live ? gv(ml.x) : -INFINITY;
    float Mx = ms;
    Mx = fmaxf(Mx, dpp<XOR2>(Mx)); Mx = fmaxf(Mx, dpp<XOR8>(Mx)); Mx = fmaxf(Mx, dpp<ROR4>(Mx));
    const float wt = __builtin_amdgcn_exp2f((ms - Mx) * LOG2E);
    float L = wt * gv(ml.z), n0 = wt * gv(a.x), n1 = wt * gv(a.z);
    L += dpp<XOR2>(L); n0 += dpp<XOR2>(n0); n1 += dpp<XOR2>(n1);
    L += dpp<XOR8>(L); n0 += dpp<XOR8>(n0); n1 += dpp<XOR8>(n1);
    L += dpp<ROR4>(L); n0 += dpp<ROR4>(n0); n1 += dpp<ROR4>(n1);
    const float inv = __builtin_amdgcn_rcpf(L);
    Y = cvtpk(n0 * inv, n1 * inv);
  }
  {
    const u32 Y0 = dppu<0xA0>(Y), Y1 = dppu<0xF5>(Y);
    const u32x4 a0 = afrag(sel, Y0 << 16), a1 = afrag(sel, Y0), a2 = afrag(sel, Y1 << 16), a3 = afrag(sel, Y1);
#pragma unroll
    for (int k = 0; k < NT; ++k) {
      f32x4 d = {0.f, 0.f, 0.f, 0.f};
      d = mfma(a0, WO[4 * k + 0], d); d = mfma(a1, WO[4 * k + 1], d); d = mfma(a2, WO[4 * k + 2], d); d = mfma(a3, WO[4 * k + 3], d);
      if (lane < 32) *(f32x4*)(lds + L_RED + ((k + 1) * NWAVE + w) * RSTR + 8 * c + 4 * g) = d;
    }
  }
  asm volatile("s_waitcnt lgkmcnt(0)\n\ts_barrier" ::: "memory");
#pragma unroll
  for (int k = 0; k < NT; ++k) {
    const f32x2 v = wg_reduce(lds + L_RED + (k + 1) * NWAVE * RSTR, w, lane);
    if ((lane & 7) == 0) st_sc1(rw, WS_Y + ((tb * 32 + h) * 640 + (tk + k) * 128 + 16 * w + 2 * (lane >> 3)) * 8, gpair(v.x, v.y, tag));
  }

  if (red) {
    const bool closer = (w == 1 && b == 0);
    u32x4 z = {0u, 0u, 0u, 0u};
    bool ny = gbad(y, tag) != 0u, nz = closer;
    for (u32 spin = 0; spin < SPIN_CAP; ++spin) {
      if (ny) y = ld_sc1(rw, yoff);
      if (nz) z = ld_sc1(rw, WS_Y + lo);
      ny = gbad(y, tag) != 0u; nz = closer && gbad(z, tag) != 0u;
      if (__builtin_amdgcn_ballot_w64(ny || nz) == 0ull) break;
    }
    float o0 = gv(y.x), o1 = gv(y.z);
    o0 += dpp<XOR2>(o0); o1 += dpp<XOR2>(o1);
    o0 += dpp<XOR8>(o0); o1 += dpp<XOR8>(o1);
    o0 += dpp<ROR4>(o0); o1 += dpp<ROR4>(o1);
    o0 = x32_sum(x16_sum(o0)); o1 = x32_sum(x16_sum(o1));
    { u32 xcc; asm volatile("s_getreg_b32 %0, hwreg(HW_REG_XCC_ID)" : "=s"(xcc)); if (((xcc >> 2) & 1) == 0) while (__builtin_amdgcn_s_memrealtime() - tpad0 < 4000ull) __builtin_amdgcn_s_sleep(1); }
    if (lane < 2) __hip_atomic_store((GAS u32*)out + quad * 2 + lane, cvtpk(o0, o1), __ATOMIC_RELAXED, __HIP_MEMORY_SCOPE_AGENT);
    if (closer && lane == 0) __hip_atomic_store((GAS u64*)((char*)ws + WS_EPOCH), ep, __ATOMIC_RELAXED, __HIP_MEMORY_SCOPE_AGENT);
  }
}

__global__ void __launch_bounds__(NTHR, 2) fused_decode(Params p) {
  __shared__ __attribute__((aligned(16))) float lds[L_TOT];
  const u64 tpad0 = __builtin_amdgcn_s_memrealtime();
  const int b = blockIdx.x, role = b >> 5, h = b & 31;
  const int lane = threadIdx.x & 63, w = __builtin_amdgcn_readfirstlane(threadIdx.x >> 6);
  const bool isA = role >= 6;
  const int nq = isA ? 8 : 4;
  const int qrow0 = ((0x20443210u >> (4 * role)) & 15) * 128;
  const int blk0 = (int)((0x100069543F2A1500ull >> (8 * role)) & 255) + h * STG, msk = isA ? 31 : 127;
  const int r0 = qrow0 + (w * 4 + (lane >> 4)) * nq;
  const u32 kvo = (u32)h * (4096 * 256) + (isA ? 3072 * 256 : 0);
  int xo = r0 * 2, xo2 = r0 * 2 + 8, qo = r0 * 8192 + (lane & 15) * 16, ko = lane * 16;
  int so0 = kvo + kv_off(blk0 & msk, w), so1 = kvo + kv_off((blk0 + 1) & msk, w), sq0 = h * 256, sq1 = h * 256 + 8192, role2 = role;
  asm volatile("" : "+v"(xo), "+v"(xo2), "+v"(qo), "+v"(ko), "+s"(so0), "+s"(so1), "+s"(sq0), "+s"(sq1), "+s"(role2));
  SB();
  const bool isA2 = role2 >= 6, tB2 = role2 < 5;
  const u16* kp = isA2 ? p.kA : p.kB;
  const u16* vp = isA2 ? p.vA : p.vB;
  const u16* wqp = tB2 ? p.wqB : p.wqA;
  const u16* xp = tB2 ? p.xB : p.xA;
  const u16* wop = tB2 ? p.woB : p.woA;
  u32* out = p.out;
  void* ws = p.ws;
  asm volatile("" : "+s"(kp), "+s"(vp), "+s"(wqp), "+s"(wop), "+s"(xp), "+s"(out), "+s"(ws));
  Pre pre;
  {
    const auto rq = RSRC(wqp, 640 * 8192);
    const auto rx = RSRC(xp, 1280);
    const auto rk = RSRC(kp, 32 * 4096 * 256);
    pre.xa = __builtin_amdgcn_raw_buffer_load_b64(rx, xo, 0, 0);
    pre.xb = __builtin_amdgcn_raw_buffer_load_b64(rx, xo2, 0, 0);
    pre.ep = __builtin_amdgcn_raw_buffer_load_b64(RSRC(ws, 64), WS_EPOCH, 0, AUX_SC1);
    SB();
    pre.q0 = ld_nt(rq, qo, sq0);
    pre.q1 = ld_nt(rq, qo, sq1);
    pre.k0 = ld_nt(rk, ko, so0);
    pre.k1 = ld_nt(rk, ko, so1);
    SB();
  }
  if (isA) body<true>(kp, vp, wqp, wop, out, ws, lds, role, h, w, lane, pre, tpad0);
  else body<false>(kp, vp, wqp, wop, out, ws, lds, role, h, w, lane, pre, tpad0);
}

extern "C" void kernel_launch(void* const* d_in, const int* in_sizes, int n_in, void* d_out, int out_size, void* d_ws, size_t ws_size,
                              hipStream_t stream) {
  Params p;
  p.xA = (const u16*)d_in[0]; p.kA = (const u16*)d_in[1]; p.vA = (const u16*)d_in[2]; p.wqA = (const u16*)d_in[3]; p.woA = (const u16*)d_in[4];
  p.xB = (const u16*)d_in[5]; p.kB = (const u16*)d_in[6]; p.vB = (const u16*)d_in[7]; p.wqB = (const u16*)d_in[8]; p.woB = (const u16*)d_in[9];
  p.out = (u32*)d_out; p.ws = d_ws;
  fused_decode<<<256, NTHR, 0, stream>>>(p);
}
```

```cpp
#include <hip/hip_runtime.h>
#include <hip/hip_bf16.h>
#include <stdint.h>

typedef unsigned int u32;
typedef unsigned short u16;
typedef unsigned long long u64;
typedef u32 u32x2 __attribute__((ext_vector_type(2)));
typedef u32 u32x4 __attribute__((ext_vector_type(4)));
typedef float f32x2 __attribute__((ext_vector_type(2)));
typedef float f32x4 __attribute__((ext_vector_type(4)));
typedef __bf16 bf16x2 __attribute__((ext_vector_type(2)));
typedef __bf16 bf16x8 __attribute__((ext_vector_type(8)));
#define LAS __attribute__((address_space(3)))
#define GAS __attribute__((address_space(1)))

constexpr int NWAVE = 8, NTHR = NWAVE * 64;
constexpr int WIN = 5;
constexpr int STG = 4;
constexpr int OOB = 0x7fffff00;
constexpr int AUX_NT = 2, AUX_SC1 = 16;
constexpr u32 SPIN_CAP = 200000;
constexpr float LOG2E = 1.4426950408889634f;

constexpr int WS_EPOCH = 0;
constexpr int WS_Q = 4096;
constexpr int WS_P = WS_Q + 64 * 6 * 1024;
constexpr int WS_ML = WS_P + 64 * 8 * 1024;
constexpr int WS_Y = WS_ML + 64 * 8 * 128;
constexpr int WS_END = WS_Y + 64 * 640 * 8;

struct Params {
  const u16 *xA, *kA, *vA, *wqA, *woA, *xB, *kB, *vB, *wqB, *woB;
  u32* out;
  void* ws;
};

#define RSRC(ptr, n) __builtin_amdgcn_make_buffer_rsrc((void*)(ptr), 0, (n), 0x00020000)
#define SB() __builtin_amdgcn_sched_barrier(0)
#define WAITVM(N) __builtin_amdgcn_s_waitcnt(((N) & 15) | (7 << 4) | (15 << 8) | (((N) >> 4) << 14))

__device__ __forceinline__ u32x4 ld_nt(__amdgpu_buffer_rsrc_t r, int off, int soff = 0) { return __builtin_amdgcn_raw_buffer_load_b128(r, off, soff, AUX_NT); }
__device__ __forceinline__ u32x4 ld_sc1(__amdgpu_buffer_rsrc_t r, int off) {
  asm volatile("" ::: "memory");
  return __builtin_amdgcn_raw_buffer_load_b128(r, off, 0, AUX_SC1);
}
__device__ __forceinline__ void st_sc1(__amdgpu_buffer_rsrc_t r, int off, u32x4 v) { __builtin_amdgcn_raw_buffer_store_b128(v, r, off, 0, AUX_SC1); }
__device__ __forceinline__ float asf(u32 x) { return __builtin_bit_cast(float, x); }
__device__ __forceinline__ u32 asu(float x) { return __builtin_bit_cast(u32, x); }
__device__ __forceinline__ u32 cvtpk(float lo, float hi) {
  f32x2 v = {lo, hi};
  return __builtin_bit_cast(u32, __builtin_convertvector(v, bf16x2));
}
__device__ __forceinline__ float dot2(u32 a, u32 b, float c) {
  return __builtin_amdgcn_fdot2_f32_bf16(__builtin_bit_cast(bf16x2, a), __builtin_bit_cast(bf16x2, b), c, false);
}
template <int CTRL> __device__ __forceinline__ float dpp(float x) { return asf((u32)__builtin_amdgcn_mov_dpp((int)asu(x), CTRL, 0xf, 0xf, true)); }
template <int CTRL> __device__ __forceinline__ u32 dppu(u32 x) { return (u32)__builtin_amdgcn_mov_dpp((int)x, CTRL, 0xf, 0xf, true); }
constexpr int XOR1 = 0xB1, XOR2 = 0x4E, XOR7 = 0x141, XOR8 = 0x128, ROR4 = 0x124, BCAST = 0x150;
__device__ __forceinline__ float x16_sum(float x) { auto s = __builtin_amdgcn_permlane16_swap(asu(x), asu(x), false, false); return asf(s[0]) + asf(s[1]); }
__device__ __forceinline__ float x32_sum(float x) { auto s = __builtin_amdgcn_permlane32_swap(asu(x), asu(x), false, false); return asf(s[0]) + asf(s[1]); }
__device__ __forceinline__ float x16_max(float x) { auto s = __builtin_amdgcn_permlane16_swap(asu(x), asu(x), false, false); return fmaxf(asf(s[0]), asf(s[1])); }
__device__ __forceinline__ float x32_max(float x) { auto s = __builtin_amdgcn_permlane32_swap(asu(x), asu(x), false, false); return fmaxf(asf(s[0]), asf(s[1])); }
__device__ __forceinline__ f32x4 mfma(u32x4 a, u32x4 b, f32x4 c) {
  return __builtin_amdgcn_mfma_f32_16x16x32_bf16(__builtin_bit_cast(bf16x8, a), __builtin_bit_cast(bf16x8, b), c, 0, 0, 0);
}
struct Sel { u32 s[4]; };
__device__ __forceinline__ u32x4 afrag(const Sel& sl, u32 X) {
  u32x4 a;
  a.x = __builtin_amdgcn_perm(X, X, sl.s[0]); a.y = __builtin_amdgcn_perm(X, X, sl.s[1]);
  a.z = __builtin_amdgcn_perm(X, X, sl.s[2]); a.w = __builtin_amdgcn_perm(X, X, sl.s[3]);
  return a;
}
struct Tag { u32 lo8; u32 hi; };
__device__ __forceinline__ u32 gpack(float v, const Tag& t) { return __builtin_amdgcn_alignbyte(t.lo8, asu(v) + 0x80u, 1); }
__device__ __forceinline__ u32x4 gpair(float a, float b, const Tag& t) { u32x4 g = {gpack(a, t), t.hi, gpack(b, t), t.hi}; return g; }
__device__ __forceinline__ u32 gbad(u32x4 g, const Tag& t) {
  return (g.y ^ t.hi) | (g.w ^ t.hi) | ((g.x >> 24) ^ t.lo8) | ((g.z >> 24) ^ t.lo8);
}
__device__ __forceinline__ bool none(u32 bad) { return __builtin_amdgcn_ballot_w64(bad != 0u) == 0ull; }
__device__ __forceinline__ float gv(u32 x) { return asf(x << 8); }

constexpr int RSTR = 136;
constexpr int L_RED = 0;
constexpr int L_Q = 3 * NWAVE * RSTR;
constexpr int L_M = L_Q + 6 * 128;
constexpr int L_L = L_M + 8;
constexpr int L_CTL = L_L + 8;
constexpr int L_TOT = L_CTL + 4;

__device__ __forceinline__ f32x2 wg_reduce(const float* red, int w, int lane) {
  f32x2 v = *(const f32x2*)(red + (lane & 7) * RSTR + 16 * w + 2 * (lane >> 3));
  v.x += dpp<XOR1>(v.x); v.y += dpp<XOR1>(v.y);
  v.x += dpp<XOR2>(v.x); v.y += dpp<XOR2>(v.y);
  v.x += dpp<XOR7>(v.x); v.y += dpp<XOR7>(v.y);
  return v;
}

__device__ __forceinline__ int kv_off(int j, int w) { return (j >> 2) * 32768 + ((w + (j >> 2)) & 7) * 4096 + (j & 3) * 1024; }

struct Pre { u32x2 xa, xb, ep; u32x4 q0, q1, k0, k1; };

template <bool ISA>
__device__ __forceinline__ void body(const u16* kp, const u16* vp, const u16* wqp, const u16* wop, u32* out, void* ws, float* lds,
                                     const int role, const int h, const int w, const int lane, const Pre& pre) {
  constexpr int NT = ISA ? 2 : 1;
  constexpr int NO = 4 * NT;
  constexpr int NQ = 4 * NT;
  constexpr int NU = ISA ? 16 : 22;
  constexpr int NBLK = ISA ? 32 : 128;
  constexpr int br = ISA ? 0 : 1;
  const int g = lane >> 4, c = lane & 15;
  const int tb = ISA ? 0 : (role < 5 ? 1 : 0);
  const int tk = ISA ? (role - 6) * 2 : (role < 5 ? role : 4);
  const int split = ISA ? role - 6 : role;
  const int qslot = ISA ? split : (role < 5 ? role : 2);

  const auto rq = RSRC(wqp + (size_t)tk * (128 * 4096) + h * 128, NQ * 32 * 8192);
  const auto ro = RSRC(wop + (size_t)h * (128 * 640) + tk * 128, 128 * 1280);
  const size_t kvoff = (size_t)h * (4096 * 128) + (ISA ? 3072 * 128 : 0);
  const auto rk = RSRC(kp + kvoff, NBLK * 8192);
  const auto rv = RSRC(vp + kvoff, NBLK * 8192);
  const auto rw = RSRC(ws, WS_END);

  const u32 xd[4] = {pre.xa.x, pre.xa.y, pre.xb.x, pre.xb.y};
  u32x4 WQ[NQ], WO[NO], K[NU], V[NU];
  const int qoff = (w * 4 + g) * NQ * 8192 + c * 16;
  const int ooff = (w * 16 + g * 4) * 1280 + c * 16;
  const int koff = lane * 16;
  const int blk0 = split * (ISA ? 16 : 21) + h * STG;
  const int xe = w * 6 + split;
  const bool xlive = !ISA && xe < 16;
  const int xso = kv_off((126 + (xe >> 3) + h * STG) & 127, xe & 7), xvo = xlive ? koff : OOB;
  constexpr int I_K = NQ + 2, I_V = NQ + NU, I_END = I_V + NU;
  WQ[0] = pre.q0; WQ[1] = pre.q1; K[0] = pre.k0; K[1] = pre.k1;
  auto issue = [&](int i) {
    if (i < I_K) WQ[i - 2] = ld_nt(rq, qoff + (i - 2) * 8192);
    else if (!ISA && i == I_V - 1) K[NU - 1] = ld_nt(rk, xvo, xso);
    else if (!ISA && i == I_END - 1) V[NU - 1] = ld_nt(rv, xvo, xso);
    else if (i < I_V) K[i - NQ] = ld_nt(rk, koff, kv_off((blk0 + i - NQ) & (NBLK - 1), w));
    else if (i < I_END) V[i - I_V] = ld_nt(rv, koff, kv_off((blk0 + i - I_V) & (NBLK - 1), w));
    SB();
  };
  auto step = [&](int i) { if (i < I_END) { WAITVM(WIN - 1); issue(i); } };
#pragma unroll
  for (int i = 4; i < WIN; ++i) issue(i);
  u32 pt = 0;
  if (w < 3) {
    const char* a = w == 0 ? (const char*)(vp + kvoff) : w == 1 ? (const char*)(wop + (size_t)h * (128 * 640)) : (const char*)out;
    const u32 o = w == 1 ? (lane & 1) * (128 * 1280 - 64) : 0u;
    if (lane < 2) pt = __hip_atomic_load((const GAS u32*)(a + o), __ATOMIC_RELAXED, __HIP_MEMORY_SCOPE_AGENT);
  }
  SB();
  Sel sel;
  {
    const u32 src = (c & 8) ? 0x0100u : 0x0302u;
    const u32 m = (c & 1) ? ((src << 16) | 0x0c0cu) : (0x0c0c0000u | src);
#pragma unroll
    for (int i = 0; i < 4; ++i) sel.s[i] = (((c & 7) >> 1) == i) ? m : 0x0c0c0c0cu;
  }
  volatile LAS u32* ctl = (volatile LAS u32*)lds + L_CTL;
  if (threadIdx.x < 4) ctl[threadIdx.x] = 0u;
  asm volatile("s_waitcnt lgkmcnt(0)\n\ts_barrier" ::: "memory");
  SB();

  f32x4 acc = {0.f, 0.f, 0.f, 0.f};
  constexpr int I_S = I_K + WIN;
#pragma unroll
  for (int i = 0; i < I_K; ++i) {
    WAITVM(WIN - 1);
    if (i < 2 || i >= 4) {
      const int t = i < 2 ? i : i - 2;
      const u32 X = (t & 1) ? xd[t >> 1] : (xd[t >> 1] << 16);
      acc = mfma(afrag(sel, X), WQ[t], acc);
    }
    SB();
    issue(i + WIN);
  }
  if (lane < 32) *(f32x4*)(lds + L_RED + w * RSTR + 8 * c + 4 * g) = acc;
  asm volatile("s_waitcnt lgkmcnt(0)" ::: "memory");
  if (lane == 0) __hip_atomic_fetch_add((LAS u32*)ctl + 1, 1u, __ATOMIC_RELAXED, __HIP_MEMORY_SCOPE_WORKGROUP);
  SB();
  const u64 ep = (((u64)__builtin_amdgcn_readfirstlane(pre.ep.y) << 32) | (u32)__builtin_amdgcn_readfirstlane(pre.ep.x)) + 1ull;
  Tag tag; tag.lo8 = (u32)ep & 0xffu; tag.hi = (u32)(ep >> 8);
  SB();

  constexpr int NQP = ISA ? 3 : 5;
  constexpr int I_P = I_S + 2;
  constexpr int I_G = I_V - (ISA ? 2 : 6);
  constexpr int I_GC = I_G + WIN;
  constexpr int I_QK = I_V + (ISA ? 7 : WIN);
  static_assert(I_P < I_G && I_GC < I_QK && I_QK >= I_V + WIN, "order");
  u32x4 G;
  const int goff = WS_Q + ((br * 32 + h) * 6 + w) * 1024 + lane * 16;
#pragma unroll
  for (int i = I_S; i < I_QK; ++i) {
    step(i);
    if (i == I_P) {
      while (ctl[1] != (u32)NWAVE) __builtin_amdgcn_s_sleep(1);
      asm volatile("" ::: "memory");
      const f32x2 v = wg_reduce(lds + L_RED, w, lane);
      if ((lane & 7) == 0) st_sc1(rw, WS_Q + ((tb * 32 + h) * 6 + qslot) * 1024 + (16 * w + 2 * (lane >> 3)) * 8, gpair(v.x, v.y, tag));
      asm volatile("" :: "v"(pt));
      SB();
    }
    if (i == I_G && w < NQP) { G = ld_sc1(rw, goff); SB(); }
    if (i == I_GC && w < NQP) {
      if (!none(gbad(G, tag))) {
        for (u32 spin = 0; spin < SPIN_CAP; ++spin) {
          G = ld_sc1(rw, goff);
          if (none(gbad(G, tag))) break;
          __builtin_amdgcn_s_sleep(4);
        }
      }
      *(f32x2*)(lds + L_Q + w * 128 + 2 * lane) = f32x2{gv(G.x), gv(G.z)};
      asm volatile("s_waitcnt lgkmcnt(0)" ::: "memory");
      if (lane == 0) __hip_atomic_fetch_add((LAS u32*)ctl, 1u, __ATOMIC_RELAXED, __HIP_MEMORY_SCOPE_WORKGROUP);
      SB();
    }
  }
  while (ctl[0] != (u32)NQP) __builtin_amdgcn_s_sleep(1);
  asm volatile("" ::: "memory");
  u32x4 q;
  {
    f32x4 qa = *(const f32x4*)(lds + L_Q + 8 * c), qb = *(const f32x4*)(lds + L_Q + 8 * c + 4);
#pragma unroll
    for (int j = 1; j < NQP; ++j) { qa += *(const f32x4*)(lds + L_Q + j * 128 + 8 * c); qb += *(const f32x4*)(lds + L_Q + j * 128 + 8 * c + 4); }
    q.x = cvtpk(qa.x, qa.y); q.y = cvtpk(qa.z, qa.w); q.z = cvtpk(qb.x, qb.y); q.w = cvtpk(qb.z, qb.w);
  }

  float x[NU];
  constexpr int QPS = 4;
  constexpr int NQS = (NU + QPS - 1) / QPS;
#pragma unroll
  for (int s = 0; s < NQS; ++s) {
    step(I_QK + s);
#pragma unroll
    for (int t = s * QPS; t < (s + 1) * QPS && t < NU; ++t)
      x[t] = dot2(K[t].w, q.w, dot2(K[t].z, q.z, dot2(K[t].y, q.y, dot2(K[t].x, q.x, 0.f))));
    SB();
  }
  constexpr int I_R = I_QK + NQS;
  step(I_R);
  const bool b3 = c & 8, b2 = c & 4, b1 = c & 2, b0 = c & 1;
  float s0, s1 = -INFINITY;
  {
    float y[8];
#pragma unroll
    for (int i = 0; i < 8; ++i) y[i] = (b3 ? x[i + 8] : x[i]) + dpp<XOR8>(b3 ? x[i] : x[i + 8]);
#pragma unroll
    for (int i = 0; i < 4; ++i) y[i] = (b2 ? y[i + 4] : y[i]) + dpp<XOR7>(b2 ? y[i] : y[i + 4]);
#pragma unroll
    for (int i = 0; i < 2; ++i) y[i] = (b1 ? y[i + 2] : y[i]) + dpp<XOR2>(b1 ? y[i] : y[i + 2]);
    s0 = (b0 ? y[1] : y[0]) + dpp<XOR1>(b0 ? y[0] : y[1]);
  }
  SB();
  step(I_R + 1);
  if constexpr (NU > 16) {
    float y[4];
#pragma unroll
    for (int i = 0; i < 4; ++i) {
      if (20 + i < NU) y[i] = (b3 ? x[20 + i] : x[16 + i]) + dpp<XOR8>(b3 ? x[16 + i] : x[20 + i]);
      else y[i] = x[16 + i] + dpp<XOR8>(x[16 + i]);
    }
#pragma unroll
    for (int i = 0; i < 2; ++i) y[i] = (b2 ? y[i + 2] : y[i]) + dpp<XOR7>(b2 ? y[i] : y[i + 2]);
    const float z = (b1 ? y[1] : y[0]) + dpp<XOR2>(b1 ? y[0] : y[1]);
    s1 = z + dpp<XOR1>(z);
  }
  {
    const u32 r = cvtpk(s0, s1);
    const u32 r2 = cvtpk(asf(r << 16) * 0.08838834764831845f, asf(r & 0xffff0000u) * 0.08838834764831845f);
    s0 = asf(r2 << 16); s1 = asf(r2 & 0xffff0000u);
    if ((c >> 1) >= (xlive ? 6 : ISA ? 0 : 5)) s1 = -INFINITY;
  }
  float m = fmaxf(s0, s1);
  m = fmaxf(m, dpp<XOR1>(m)); m = fmaxf(m, dpp<XOR2>(m)); m = fmaxf(m, dpp<XOR7>(m)); m = fmaxf(m, dpp<XOR8>(m));
  m = x32_max(x16_max(m));
  if (lane == 0) {
    lds[L_M + w] = m;
    asm volatile("s_waitcnt lgkmcnt(0)" ::: "memory");
    __hip_atomic_fetch_add((LAS u32*)ctl + 2, 1u, __ATOMIC_RELAXED, __HIP_MEMORY_SCOPE_WORKGROUP);
  }
  const float p0 = __builtin_amdgcn_exp2f((s0 - m) * LOG2E), p1 = __builtin_amdgcn_exp2f((s1 - m) * LOG2E);
  const u32 P0 = cvtpk(p0 - asf(cvtpk(p0, p0) & 0xffff0000u), p0);
  const u32 P1 = cvtpk(p1 - asf(cvtpk(p1, p1) & 0xffff0000u), p1);
  float l = p0 + (b0 ? 0.f : p1);
  l += dpp<XOR1>(l); l += dpp<XOR2>(l); l += dpp<XOR7>(l); l += dpp<XOR8>(l);
  l = x32_sum(x16_sum(l));
  SB();
  const bool live = (c >> 1) < (tb ? 6 : 2);
  const int aoff = WS_P + ((tb * 32 + h) * 8 + (c >> 1)) * 1024 + (16 * w + 4 * g + 2 * (c & 1)) * 8;
  const int moff = WS_ML + ((tb * 32 + h) * 8 + (c >> 1)) * 128;
  const int b = blockIdx.x;
  const bool red = (w == 0 || (w == 1 && b < 64));
  const int quad = w == 0 ? b : 256 + b;
  const int lo = (lane >> 1) * (640 * 8) + (lane & 1) * 16;
  const int yoff = WS_Y + (quad >= 160 ? 32 * 640 * 8 + (quad - 160) * 32 : quad * 32) + lo;
  u32x4 a = ld_sc1(rw, live ? aoff : OOB), ml = ld_sc1(rw, live ? moff : OOB), y = {0u, 0u, 0u, 0u};
  if (red) y = ld_sc1(rw, yoff);
  SB();

  acc = f32x4{0.f, 0.f, 0.f, 0.f};
  constexpr int I_PV = I_R + 2, NPS = I_END - I_PV;
  constexpr int NEARLY = NU - WIN;
  static_assert(NPS >= 0 && NPS <= NEARLY, "every remaining load gets its step");
  float M = 0.f, sc = 0.f;
#define PV(t) if constexpr ((t) < NU) { \
    if constexpr ((t) < NEARLY && ((t) * NPS) % NEARLY < NPS) { SB(); step(I_PV + ((t) * NPS) / NEARLY); } \
    if constexpr ((t) == NEARLY) { \
      SB(); \
      while (ctl[2] != (u32)NWAVE) __builtin_amdgcn_s_sleep(1); \
      asm volatile("" ::: "memory"); \
      const f32x4 ma = *(const f32x4*)(lds + L_M), mb = *(const f32x4*)(lds + L_M + 4); \
      M = fmaxf(fmaxf(fmaxf(ma.x, ma.y), fmaxf(ma.z, ma.w)), fmaxf(fmaxf(mb.x, mb.y), fmaxf(mb.z, mb.w))); \
      sc = __builtin_amdgcn_exp2f((m - M) * LOG2E); \
      if (lane == 0) lds[L_L + w] = l * sc; \
      SB(); \
    } \
    const u32 X = (t) < 16 ? dppu<BCAST + ((t) & 15)>(P0) : dppu<BCAST + ((2 * (t)) & 15)>(P1); \
    acc = mfma(afrag(sel, X), V[t], acc); \
    if constexpr ((t) == NU - 2) { \
      SB(); \
      _Pragma("unroll") for (int o = 0; o < NO; ++o) WO[o] = ld_nt(ro, ooff + (o & 3) * 1280 + (o >> 2) * 256); \
      SB(); \
    } }
  PV(0) PV(1) PV(2) PV(3) PV(4) PV(5) PV(6) PV(7) PV(8) PV(9) PV(10) PV(11) PV(12) PV(13) PV(14) PV(15) PV(16) PV(17) PV(18) PV(19) PV(20) PV(21)
#undef PV
  {
    auto u = __builtin_amdgcn_permlane32_swap(asu(acc.x), asu(acc.z), false, false);
    auto v = __builtin_amdgcn_permlane32_swap(asu(acc.y), asu(acc.w), false, false);
    const f32x2 r = {(asf(u[0]) + asf(u[1])) * sc, (asf(v[0]) + asf(v[1])) * sc};
    *(f32x2*)(lds + L_RED + w * RSTR + 8 * c + 4 * (g & 1) + 2 * (g >> 1)) = r;
  }
  asm volatile("s_waitcnt lgkmcnt(0)\n\ts_barrier" ::: "memory");
  {
    const f32x2 v = wg_reduce(lds + L_RED, w, lane);
    float L = lds[L_L + (lane & 7)];
    L += dpp<XOR1>(L); L += dpp<XOR2>(L); L += dpp<XOR7>(L);
    const int slot = (br * 32 + h) * 8 + split;
    if ((lane & 7) == 0) st_sc1(rw, WS_P + slot * 1024 + (16 * w + 2 * (lane >> 3)) * 8, gpair(v.x, v.y, tag));
    if (threadIdx.x < 8) st_sc1(rw, WS_ML + slot * 128 + lane * 16, gpair(M, L, tag));
  }

  u32 Y;
  {
    bool na = live && gbad(a, tag) != 0u, nm = live && gbad(ml, tag) != 0u;
    for (u32 spin = 0; spin < SPIN_CAP; ++spin) {
      if (na) a = ld_sc1(rw, aoff);
      if (nm) ml = ld_sc1(rw, moff);
      na = live && gbad(a, tag) != 0u; nm = live && gbad(ml, tag) != 0u;
      if (__builtin_amdgcn_ballot_w64(na || nm) == 0ull) break;
    }
    const float ms = live ? gv(ml.x) : -INFINITY;
    float Mx = ms;
    Mx = fmaxf(Mx, dpp<XOR2>(Mx)); Mx = fmaxf(Mx, dpp<XOR8>(Mx)); Mx = fmaxf(Mx, dpp<ROR4>(Mx));
    const float wt = __builtin_amdgcn_exp2f((ms - Mx) * LOG2E);
    float L = wt * gv(ml.z), n0 = wt * gv(a.x), n1 = wt * gv(a.z);
    L += dpp<XOR2>(L); n0 += dpp<XOR2>(n0); n1 += dpp<XOR2>(n1);
    L += dpp<XOR8>(L); n0 += dpp<XOR8>(n0); n1 += dpp<XOR8>(n1);
    L += dpp<ROR4>(L); n0 += dpp<ROR4>(n0); n1 += dpp<ROR4>(n1);
    const float inv = __builtin_amdgcn_rcpf(L);
    Y = cvtpk(n0 * inv, n1 * inv);
  }
  {
    const u32 Y0 = dppu<0xA0>(Y), Y1 = dppu<0xF5>(Y);
    const u32x4 a0 = afrag(sel, Y0 << 16), a1 = afrag(sel, Y0), a2 = afrag(sel, Y1 << 16), a3 = afrag(sel, Y1);
#pragma unroll
    for (int k = 0; k < NT; ++k) {
      f32x4 d = {0.f, 0.f, 0.f, 0.f};
      d = mfma(a0, WO[4 * k + 0], d); d = mfma(a1, WO[4 * k + 1], d); d = mfma(a2, WO[4 * k + 2], d); d = mfma(a3, WO[4 * k + 3], d);
      if (lane < 32) *(f32x4*)(lds + L_RED + ((k + 1) * NWAVE + w) * RSTR + 8 * c + 4 * g) = d;
    }
  }
  asm volatile("s_waitcnt lgkmcnt(0)\n\ts_barrier" ::: "memory");
#pragma unroll
  for (int k = 0; k < NT; ++k) {
    const f32x2 v = wg_reduce(lds + L_RED + (k + 1) * NWAVE * RSTR, w, lane);
    if ((lane & 7) == 0) st_sc1(rw, WS_Y + ((tb * 32 + h) * 640 + (tk + k) * 128 + 16 * w + 2 * (lane >> 3)) * 8, gpair(v.x, v.y, tag));
  }

  if (red) {
    const bool closer = (w == 1 && b == 0);
    u32x4 z = {0u, 0u, 0u, 0u};
    bool ny = gbad(y, tag) != 0u, nz = closer;
    for (u32 spin = 0; spin < SPIN_CAP; ++spin) {
      if (ny) y = ld_sc1(rw, yoff);
      if (nz) z = ld_sc1(rw, WS_Y + lo);
      ny = gbad(y, tag) != 0u; nz = closer && gbad(z, tag) != 0u;
      if (__builtin_amdgcn_ballot_w64(ny || nz) == 0ull) break;
    }
    float o0 = gv(y.x), o1 = gv(y.z);
    o0 += dpp<XOR2>(o0); o1 += dpp<XOR2>(o1);
    o0 += dpp<XOR8>(o0); o1 += dpp<XOR8>(o1);
    o0 += dpp<ROR4>(o0); o1 += dpp<ROR4>(o1);
    o0 = x32_sum(x16_sum(o0)); o1 = x32_sum(x16_sum(o1));
    if (lane < 2) __hip_atomic_store((GAS u32*)out + quad * 2 + lane, cvtpk(o0, o1), __ATOMIC_RELAXED, __HIP_MEMORY_SCOPE_AGENT);
    if (closer && lane == 0) __hip_atomic_store((GAS u64*)((char*)ws + WS_EPOCH), ep, __ATOMIC_RELAXED, __HIP_MEMORY_SCOPE_AGENT);
  }
}

__global__ void __launch_bounds__(NTHR, 2) fused_decode(Params p) {
  __shared__ __attribute__((aligned(16))) float lds[L_TOT];
  const int b = blockIdx.x, role = b >> 5, h = b & 31;
  const int lane = threadIdx.x & 63, w = __builtin_amdgcn_readfirstlane(threadIdx.x >> 6);
  const bool isA = role >= 6;
  const int nq = isA ? 8 : 4;
  const int qrow0 = ((0x20443210u >> (4 * role)) & 15) * 128;
  const int blk0 = (int)((0x100069543F2A1500ull >> (8 * role)) & 255) + h * STG, msk = isA ? 31 : 127;
  const int r0 = qrow0 + (w * 4 + (lane >> 4)) * nq;
  const u32 kvo = (u32)h * (4096 * 256) + (isA ? 3072 * 256 : 0);
  int xo = r0 * 2, xo2 = r0 * 2 + 8, qo = r0 * 8192 + (lane & 15) * 16, ko = lane * 16;
  int so0 = kvo + kv_off(blk0 & msk, w), so1 = kvo + kv_off((blk0 + 1) & msk, w), sq0 = h * 256, sq1 = h * 256 + 8192, role2 = role;
  asm volatile("" : "+v"(xo), "+v"(xo2), "+v"(qo), "+v"(ko), "+s"(so0), "+s"(so1), "+s"(sq0), "+s"(sq1), "+s"(role2));
  SB();
  const bool isA2 = role2 >= 6, tB2 = role2 < 5;
  const u16* kp = isA2 ? p.kA : p.kB;
  const u16* vp = isA2 ? p.vA : p.vB;
  const u16* wqp = tB2 ? p.wqB : p.wqA;
  const u16* xp = tB2 ? p.xB : p.xA;
  const u16* wop = tB2 ? p.woB : p.woA;
  u32* out = p.out;
  void* ws = p.ws;
  asm volatile("" : "+s"(kp), "+s"(vp), "+s"(wqp), "+s"(wop), "+s"(xp), "+s"(out), "+s"(ws));
  Pre pre;
  {
    const auto rq = RSRC(wqp, 640 * 8192);
    const auto rx = RSRC(xp, 1280);
    const auto rk = RSRC(kp, 32 * 4096 * 256);
    pre.xa = __builtin_amdgcn_raw_buffer_load_b64(rx, xo, 0, 0);
    pre.xb = __builtin_amdgcn_raw_buffer_load_b64(rx, xo2, 0, 0);
    pre.ep = __builtin_amdgcn_raw_buffer_load_b64(RSRC(ws, 64), WS_EPOCH, 0, AUX_SC1);
    SB();
    pre.q0 = ld_nt(rq, qo, sq0);
    pre.q1 = ld_nt(rq, qo, sq1);
    pre.k0 = ld_nt(rk, ko, so0);
    pre.k1 = ld_nt(rk, ko, so1);
    SB();
  }
  if (isA) body<true>(kp, vp, wqp, wop, out, ws, lds, role, h, w, lane, pre);
  else body<false>(kp, vp, wqp, wop, out, ws, lds, role, h, w, lane, pre);
}

extern "C" void kernel_launch(void* const* d_in, const int* in_sizes, int n_in, void* d_out, int out_size, void* d_ws, size_t ws_size,
                              hipStream_t stream) {
  Params p;
  p.xA = (const u16*)d_in[0]; p.kA = (const u16*)d_in[1]; p.vA = (const u16*)d_in[2]; p.wqA = (const u16*)d_in[3]; p.woA = (const u16*)d_in[4];
  p.xB = (const u16*)d_in[5]; p.kB = (const u16*)d_in[6]; p.vB = (const u16*)d_in[7]; p.wqB = (const u16*)d_in[8]; p.woB = (const u16*)d_in[9];
  p.out = (u32*)d_out; p.ws = d_ws;
  fused_decode<<<256, NTHR, 0, stream>>>(p);
}
```
